```python
import torch

def _softmax(x, axis=-1):
    return torch.softmax(x, dim=axis)

def _gelu(x):
    return torch.nn.functional.gelu(x, approximate='tanh')

def _silu(x):
    return torch.nn.functional.silu(x)

def _sigmoid(x):
    return torch.sigmoid(x)

def _layernorm(x, axis=-1, eps=1e-05):
    m = x.mean(dim=axis, keepdim=True)
    v = x.var(dim=axis, keepdim=True, unbiased=False)
    return (x - m) / torch.sqrt(v + eps)

def _scatter_add(tgt, idx, src, axis=0):
    return tgt.clone().index_add_(axis, idx, src)

def _segment_sum(src, seg, n):
    out = torch.zeros((n,) + src.shape[1:], dtype=src.dtype)
    return out.index_add_(0, seg, src)

def _pad(x, pw):
    flat = tuple((v for pair in reversed(pw) for v in pair))
    return torch.nn.functional.pad(x, flat)
kernel_name = 'sequential_preLN__sliding_block__mixer_gmlp__prompt'

def setup_inputs():

    _w = lambda a: (lambda t: t.to(torch.bfloat16) if t.is_floating_point() else t)(torch.as_tensor(a))

    def _A_setup():
        B, S, D = (1, 2048, 512)
        return {'x': _w((lambda *s: torch.randn(*s, dtype=torch.bfloat16) * 0.05)(B, S, D)), 'wqkv': _w((lambda *s: torch.randn(*s, dtype=torch.bfloat16) * 0.05)(D, 3 * D)), 'wo': _w((lambda *s: torch.randn(*s, dtype=torch.bfloat16) * 0.05)(D, D))}
    _A_in = _A_setup()

    def _B_setup():
        B, S, D, F = (1, 2048, 512, 1792)
        Fh = F // 2
        return {'x': _w((lambda *s: torch.randn(*s, dtype=torch.bfloat16) * 0.05)(B, S, D)), 'w_in': _w((lambda *s: torch.randn(*s, dtype=torch.bfloat16) * 0.4)(D, 2 * Fh)), 'w_sp': _w(0.01 * (lambda *s: torch.randn(*s, dtype=torch.bfloat16) * 0.4)(S, S)), 'b_sp': _w(1.0 + 0.01 * (lambda *s: torch.randn(*s, dtype=torch.bfloat16) * 0.05)(S)), 'w_out': _w((lambda *s: torch.randn(*s, dtype=torch.bfloat16) * 0.05)(Fh, D))}
    _B_in = _B_setup()
    return {'A_x': _A_in['x'], 'A_wqkv': _A_in['wqkv'], 'A_wo': _A_in['wo'], 'B_x': _B_in['x'], 'B_w_in': _B_in['w_in'], 'B_w_sp': _B_in['w_sp'], 'B_b_sp': _B_in['b_sp'], 'B_w_out': _B_in['w_out']}

def reference(A_x, A_wqkv, A_wo, B_x, B_w_in, B_w_sp, B_b_sp, B_w_out):

    def _A_ref(x, wqkv, wo):
        B, S, D = x.shape
        H = 32
        Dh = D // H
        W = 256
        qkv = (lambda x, *d: x.permute(*d))((x @ wqkv).reshape(B, S, 3, H, Dh), 2, 0, 3, 1, 4)
        q, k, v = (qkv[0], qkv[1], qkv[2])
        scores = torch.einsum('bhqd,bhkd->bhqk', q, k) * Dh ** (-0.5)
        idx = torch.arange(S)
        mask = (idx[:, None] - idx[None, :] >= 0) & (idx[:, None] - idx[None, :] < W)
        scores = torch.where(mask[None, None], scores, -1000000000.0)
        p = _softmax(scores, axis=-1)
        o = torch.einsum('bhqk,bhkd->bhqd', p, v)
        return (lambda x, *d: x.permute(*d))(o, 0, 2, 1, 3).reshape(B, S, D) @ wo

    def _B_ref(x, w_in, w_sp, b_sp, w_out):
        B, S, D = x.shape
        F = w_in.shape[-1]
        Fh = F // 2
        z = _gelu(torch.einsum('bsd,df->bsf', x, w_in))
        z1 = z[..., :Fh].clone()
        z2 = _layernorm(z[..., Fh:], axis=-1)
        g = torch.einsum('ts,bsf->btf', w_sp, z2) + b_sp[None, :, None]
        return torch.einsum('bsf,fd->bsd', z1 * g, w_out)
    x = A_x
    h = x + _A_ref(_layernorm(x, axis=-1), A_wqkv, A_wo)
    h2 = h + _B_ref(_layernorm(h, axis=-1), B_w_in, B_w_sp, B_b_sp, B_w_out)
    return h2
```

```cpp
#include <hip/hip_runtime.h>
#include <hip/hip_bf16.h>
#include <type_traits>

typedef unsigned short u16;
typedef short s16x8 __attribute__((ext_vector_type(8)));
typedef short s16x4 __attribute__((ext_vector_type(4)));
typedef float f32x4 __attribute__((ext_vector_type(4)));
typedef float f32x2 __attribute__((ext_vector_type(2)));
typedef float f32x16 __attribute__((ext_vector_type(16)));
typedef __bf16 bf16x2 __attribute__((ext_vector_type(2)));
typedef __bf16 bf16x8 __attribute__((ext_vector_type(8)));
typedef unsigned u32x4 __attribute__((ext_vector_type(4)));
typedef unsigned u32x2 __attribute__((ext_vector_type(2)));

#define DI __device__ __forceinline__

constexpr int S_ = 2048, D_ = 512, H_ = 32, DH_ = 16, F_ = 1792, FH_ = 896;

DI unsigned pack2(float a, float b) {
  bf16x2 v = {(__bf16)a, (__bf16)b};
  return __builtin_bit_cast(unsigned, v);
}
DI float bflo(unsigned u) { return __uint_as_float(u << 16); }
DI float bfhi(unsigned u) { return __uint_as_float(u & 0xffff0000u); }
DI float rbf(float a) {
  __bf16 v = (__bf16)a;
  return __uint_as_float(((unsigned)__builtin_bit_cast(u16, v)) << 16);
}
DI float dot2(unsigned a, unsigned b, float c) {
  return __builtin_amdgcn_fdot2_f32_bf16(__builtin_bit_cast(bf16x2, a), __builtin_bit_cast(bf16x2, b), c, false);
}
DI f32x4 mfma16(u32x4 a, u32x4 b, f32x4 c) {
  return __builtin_amdgcn_mfma_f32_16x16x32_bf16(__builtin_bit_cast(bf16x8, a), __builtin_bit_cast(bf16x8, b), c, 0, 0, 0);
}
DI f32x16 mfma32(u32x4 a, u32x4 b, f32x16 c) {
  return __builtin_amdgcn_mfma_f32_32x32x16_bf16(__builtin_bit_cast(bf16x8, a), __builtin_bit_cast(bf16x8, b), c, 0, 0, 0);
}
template <int N> DI void wait_vm() { asm volatile("s_waitcnt vmcnt(%0)" ::"n"(N) : "memory"); }
DI void wait_lgkm0() { asm volatile("s_waitcnt lgkmcnt(0)" ::: "memory"); }
DI void bar() { __builtin_amdgcn_s_barrier(); }

template <int OFF> DI u32x2 tr_read(unsigned addr) {
  u32x2 r;
  asm volatile("ds_read_b64_tr_b16 %0, %1 offset:%2" : "=v"(r) : "v"(addr), "n"(OFF));
  return r;
}
DI u32x4 lds_read128(unsigned addr) {
  u32x4 r;
  asm volatile("ds_read_b128 %0, %1" : "=v"(r) : "v"(addr));
  return r;
}
template <int N> DI void wait_lgkm() { asm volatile("s_waitcnt lgkmcnt(%0)" ::"n"(N) : "memory"); }
template <int I, int N, class F> DI void static_for(F&& f) {
  if constexpr (I < N) {
    f(std::integral_constant<int, I>{});
    static_for<I + 1, N>(f);
  }
}
DI void glds16(const void* g, void* l) {
  __builtin_amdgcn_global_load_lds((const unsigned*)g, (unsigned*)l, 16, 0, 0);
}

enum { M_QKV = 0, M_WO = 1, M_WIN = 2, M_SP = 3, M_OUT = 4 };

template <int BM, int BN, int WM, int WN, int WK, int KT, int NS, int MODE, int LDA, int LDB, int PM, int PN>
__global__ __launch_bounds__(256, 1) void gemm_k(const u16* __restrict__ A, const u16* __restrict__ B,
                                                 const u16* __restrict__ e0, const u16* __restrict__ e1,
                                                 const float* __restrict__ partin, u16* __restrict__ o0,
                                                 u16* __restrict__ o1, u16* __restrict__ o2,
                                                 float* __restrict__ partout) {
  constexpr int AB = BM * 128, BB = BN * 128, STG = AB + BB;
  constexpr int AI = AB / 1024, BI = BB / 1024;
  static_assert(AI % 4 == 0, "A instr");
  constexpr int LA = AI / 4;
  constexpr int LBF = BI / 4, LBR = BI % 4;
  constexpr int MB = BM / WM / 16, NB = BN / WN / 16, NBT = BN / 16;
  constexpr int RING = NS * STG;
  constexpr int EXTRA = (MODE == M_SP) ? 16384 : 0;
  constexpr bool LNA = (MODE == M_QKV || MODE == M_WIN);
  __shared__ __attribute__((aligned(16))) char smem[RING + EXTRA];

  const int tid = threadIdx.x, lane = tid & 63;
  const int wid = __builtin_amdgcn_readfirstlane(tid >> 6);
  const int wk = (WK == 2) ? (wid & 1) : 0;
  const int wmn = (WK == 2) ? (wid >> 1) : wid;
  const int wm = wmn / WN, wn = wmn % WN;
  const int r16 = lane & 15, g = lane >> 4;

  constexpr int TN = (MODE == M_QKV ? 1536 : MODE == M_WIN ? 1792 : MODE == M_SP ? 896 : 512) / BN;
  constexpr int PXN = TN / PN;
  int bid = blockIdx.x, xcd = bid & 7, jj = bid >> 3;
  int tm = (xcd / PXN) * PM + jj / PN, tn = (xcd % PXN) * PN + jj % PN;
  const int m0 = tm * BM, n0 = tn * BN;

  unsigned aoff[LA], boff[LBF + 1];
#pragma unroll
  for (int i = 0; i < LA; ++i) {
    int q = wid + 4 * i;
    int row = q * 8 + (lane >> 3), pos = lane & 7;
    aoff[i] = (unsigned)((m0 + row) * LDA + ((pos ^ (row & 7)) << 3));
  }
#pragma unroll
  for (int i = 0; i < LBF + 1; ++i) {
    int q = wid + 4 * i;
    if (q >= BI) q = BI - 1;
    int p = q * 64 + lane;
    int sub = p >> 4, c = p & 15;
    int kb = sub / NBT, nb = sub % NBT;
    int row = (c >> 1) ^ ((kb & 1) << 2);
    boff[i] = (unsigned)((kb * 8 + row) * LDB + n0 + nb * 16 + (c & 1) * 8);
  }
  const bool extraB = (wid < LBR);

  auto issue = [&](int kt, int slot) {
    const u16* a = A + kt * 64;
    const u16* b = B + (size_t)kt * 64 * LDB;
    char* s = smem + slot * STG;
#pragma unroll
    for (int i = 0; i < LA; ++i) glds16(a + aoff[i], s + (wid + 4 * i) * 1024);
#pragma unroll
    for (int i = 0; i < LBF; ++i) glds16(b + boff[i], s + AB + (wid + 4 * i) * 1024);
    if (LBR != 0) {
      if (extraB) glds16(b + boff[LBF], s + AB + (wid + 4 * LBF) * 1024);
    }
  };

  if constexpr (MODE == M_SP) {
    float* sr = (float*)(smem + RING);
    float* srm = sr + 2048;
#pragma unroll
    for (int j = 0; j < 4; ++j) {
      int row = 2 * tid + 512 * j;
      f32x4 t = {0.f, 0.f, 0.f, 0.f};
#pragma unroll
      for (int p = 0; p < 8; ++p) {
        f32x4 v = *(const f32x4*)(partin + (size_t)(p * 2048 + row) * 2);
        t += v;
      }
      float mean0 = t[0] * (1.f / 896.f), mean1 = t[2] * (1.f / 896.f);
      float var0 = fmaxf(t[1] * (1.f / 896.f) - mean0 * mean0, 0.f);
      float var1 = fmaxf(t[3] * (1.f / 896.f) - mean1 * mean1, 0.f);
      float r0 = rsqrtf(var0 + 1e-5f), r1 = rsqrtf(var1 + 1e-5f);
      *(f32x2*)(sr + row) = (f32x2){r0, r1};
      *(f32x2*)(srm + row) = (f32x2){r0 * mean0, r1 * mean1};
    }
    wait_lgkm0();
  }

#pragma unroll
  for (int s = 0; s < NS - 1; ++s)
    if (s < KT) issue(s, s);

  f32x4 acc[MB][NB];
#pragma unroll
  for (int i = 0; i < MB; ++i)
#pragma unroll
    for (int j = 0; j < NB; ++j) acc[i][j] = (f32x4){0.f, 0.f, 0.f, 0.f};
  float s1[MB], s2[MB], cs[NB], cc[MB];
#pragma unroll
  for (int i = 0; i < MB; ++i) { s1[i] = 0.f; s2[i] = 0.f; cc[i] = 0.f; }
#pragma unroll
  for (int j = 0; j < NB; ++j) cs[j] = 0.f;

  unsigned ard[MB][2];
#pragma unroll
  for (int i = 0; i < MB; ++i) {
    int row = wm * (BM / WM) + i * 16 + r16;
#pragma unroll
    for (int ks = 0; ks < 2; ++ks) ard[i][ks] = row * 128 + (((4 * ks + g) ^ (row & 7)) << 4);
  }
  unsigned brd[2][2];
  {
    int q = (lane & 15) >> 2, p = lane & 3;
#pragma unroll
    for (int ks = 0; ks < 2; ++ks) {
      int kb = 4 * ks + g;
#pragma unroll
      for (int t = 0; t < 2; ++t) {
        int row = (4 * t + q) ^ ((kb & 1) << 2);
        brd[ks][t] = AB + (kb * NBT + wn * NB) * 256 + row * 32 + p * 8;
      }
    }
  }

  const unsigned lbase = (unsigned)(size_t)smem;
  auto compute = [&](int kt, int slot) {
    const unsigned sb = lbase + slot * STG;
    constexpr int NKS = 2 / WK;
    u32x4 afa[NKS][MB];
    u32x2 blo[NKS][NB], bhi[NKS][NB];
#pragma unroll
    for (int kq = 0; kq < NKS; ++kq) {
#pragma unroll
      for (int i = 0; i < MB; ++i)
        afa[kq][i] = lds_read128(sb + (WK == 2 ? (wk ? ard[i][1] : ard[i][0]) : ard[i][kq]));
      unsigned b0 = sb + (WK == 2 ? (wk ? brd[1][0] : brd[0][0]) : brd[kq][0]);
      unsigned b1 = sb + (WK == 2 ? (wk ? brd[1][1] : brd[0][1]) : brd[kq][1]);
      static_for<0, NB>([&](auto J) {
        blo[kq][J] = tr_read<J * 256>(b0);
        bhi[kq][J] = tr_read<J * 256>(b1);
      });
    }
#pragma unroll
    for (int kq = 0; kq < NKS; ++kq) {
      const int ks = (WK == 2) ? wk : kq;
      constexpr int PER = MB + 2 * NB;
      if (kq == 0 && NKS == 2) wait_lgkm<(PER > 15 ? 15 : PER)>();
      else wait_lgkm<0>();
      u32x4 af[MB], bf[NB];
#pragma unroll
      for (int i = 0; i < MB; ++i) {
        asm volatile("" : "+v"(afa[kq][i]));
        af[i] = afa[kq][i];
      }
#pragma unroll
      for (int j = 0; j < NB; ++j) {
        asm volatile("" : "+v"(blo[kq][j]), "+v"(bhi[kq][j]));
        bf[j] = (u32x4){blo[kq][j][0], blo[kq][j][1], bhi[kq][j][0], bhi[kq][j][1]};
      }
      if constexpr (MODE == M_SP) {
        const float* sr = (const float*)(smem + RING) + kt * 64 + ks * 32 + g * 8;
        f32x4 r0 = *(const f32x4*)(sr), r1 = *(const f32x4*)(sr + 4);
        f32x4 m0v = *(const f32x4*)(sr + 2048), m1v = *(const f32x4*)(sr + 2052);
#pragma unroll
        for (int i = 0; i < MB; ++i) {
          float w0 = bflo(af[i][0]), w1 = bfhi(af[i][0]), w2 = bflo(af[i][1]), w3 = bfhi(af[i][1]);
          float w4 = bflo(af[i][2]), w5 = bfhi(af[i][2]), w6 = bflo(af[i][3]), w7 = bfhi(af[i][3]);
          float c = cc[i];
          c = fmaf(w0, m0v[0], c); c = fmaf(w1, m0v[1], c); c = fmaf(w2, m0v[2], c); c = fmaf(w3, m0v[3], c);
          c = fmaf(w4, m1v[0], c); c = fmaf(w5, m1v[1], c); c = fmaf(w6, m1v[2], c); c = fmaf(w7, m1v[3], c);
          cc[i] = c;
          af[i][0] = pack2(w0 * r0[0], w1 * r0[1]);
          af[i][1] = pack2(w2 * r0[2], w3 * r0[3]);
          af[i][2] = pack2(w4 * r1[0], w5 * r1[1]);
          af[i][3] = pack2(w6 * r1[2], w7 * r1[3]);
        }
      }
      if constexpr (LNA) {
#pragma unroll
        for (int i = 0; i < MB; ++i) {
#pragma unroll
          for (int e = 0; e < 4; ++e) {
            s1[i] = dot2(af[i][e], 0x3F803F80u, s1[i]);
            s2[i] = dot2(af[i][e], af[i][e], s2[i]);
          }
        }
        if (((kt * 2 + ks) & 3) == wid) {
#pragma unroll
          for (int j = 0; j < NB; ++j)
#pragma unroll
            for (int e = 0; e < 4; ++e) cs[j] = dot2(bf[j][e], 0x3F803F80u, cs[j]);
        }
      }
#pragma unroll
      for (int i = 0; i < MB; ++i)
#pragma unroll
        for (int j = 0; j < NB; ++j) acc[i][j] = mfma16(bf[j], af[i], acc[i][j]);
    }
  };

  constexpr int NMAIN = (KT - NS + 2) > 0 ? (KT - NS + 2) : 0;
  int slot = 0;
  for (int kt = 0; kt < NMAIN; ++kt) {
    if (LBR != 0 && extraB) wait_vm<(NS - 2) * (LA + LBF + 1)>();
    else wait_vm<(NS - 2) * (LA + LBF)>();
    bar();
    int ns = slot - 1; if (ns < 0) ns += NS;
    if (kt + NS - 1 < KT) issue(kt + NS - 1, ns);
    compute(kt, slot);
    ++slot; if (slot == NS) slot = 0;
  }
#define TAILSTEP(NL)                                                     \
  if constexpr ((NL) <= NS - 3 && KT - 1 - (NL) >= NMAIN) {              \
    if (LBR != 0 && extraB) wait_vm<(NL) * (LA + LBF + 1)>();            \
    else wait_vm<(NL) * (LA + LBF)>();                                   \
    bar();                                                               \
    compute(KT - 1 - (NL), slot);                                        \
    ++slot; if (slot == NS) slot = 0;                                    \
  }
  TAILSTEP(6) TAILSTEP(5) TAILSTEP(4) TAILSTEP(3) TAILSTEP(2) TAILSTEP(1) TAILSTEP(0)
#undef TAILSTEP
  static_assert(NS - 3 <= 6, "tail");

  const int tokb = m0 + wm * (BM / WM);
  const int nbase = n0 + wn * (BN / WN);

  if constexpr (LNA) {
    wait_lgkm0();
    bar();
    float* csl = (float*)smem;
#pragma unroll
    for (int j = 0; j < NB; ++j) {
      float v = cs[j];
      v += __shfl_xor(v, 16);
      v += __shfl_xor(v, 32);
      if (g == 0) csl[wid * BN + j * 16 + r16] = v;
    }
    float mean[MB], rstd[MB];
#pragma unroll
    for (int i = 0; i < MB; ++i) {
      float a = s1[i], b = s2[i];
      a += __shfl_xor(a, 16); a += __shfl_xor(a, 32);
      b += __shfl_xor(b, 16); b += __shfl_xor(b, 32);
      mean[i] = a * (1.f / 512.f);
      float var = fmaxf(b * (1.f / 512.f) - mean[i] * mean[i], 0.f);
      rstd[i] = rsqrtf(var + 1e-5f);
    }
    wait_lgkm0();
    bar();
    float ps1[MB], ps2[MB];
#pragma unroll
    for (int i = 0; i < MB; ++i) { ps1[i] = 0.f; ps2[i] = 0.f; }
#pragma unroll
    for (int j = 0; j < NB; ++j) {
      f32x4 c4 = *(const f32x4*)(csl + j * 16 + 4 * g);
      c4 += *(const f32x4*)(csl + BN + j * 16 + 4 * g);
      c4 += *(const f32x4*)(csl + 2 * BN + j * 16 + 4 * g);
      c4 += *(const f32x4*)(csl + 3 * BN + j * 16 + 4 * g);
      const int n = nbase + j * 16;
      if constexpr (MODE == M_QKV) {
        const int which = n >> 9, head = (n & 511) >> 4;
        if (which < 2) {
          u16* dst = (which == 0 ? o0 : o1);
#pragma unroll
          for (int i = 0; i < MB; ++i) {
            int tok = tokb + i * 16 + r16;
            float v0 = rstd[i] * (acc[i][j][0] - mean[i] * c4[0]);
            float v1 = rstd[i] * (acc[i][j][1] - mean[i] * c4[1]);
            float v2 = rstd[i] * (acc[i][j][2] - mean[i] * c4[2]);
            float v3 = rstd[i] * (acc[i][j][3] - mean[i] * c4[3]);
            *(u32x2*)(dst + ((size_t)head * S_ + tok) * 16 + 4 * g) = (u32x2){pack2(v0, v1), pack2(v2, v3)};
          }
        } else {
          u16* vt = (u16*)(smem + 4096 + (wid * NB + j) * (MB * 512));
#pragma unroll
          for (int i = 0; i < MB; ++i) {
#pragma unroll
            for (int e = 0; e < 4; ++e) {
              float v = rstd[i] * (acc[i][j][e] - mean[i] * c4[e]);
              __bf16 bv = (__bf16)v;
              vt[(4 * g + e) * (MB * 16) + i * 16 + r16] = __builtin_bit_cast(u16, bv);
            }
          }
          static_assert(MB == 2, "V transpose assumes 32 tokens per wave");
          int d = lane >> 2, c = lane & 3;
          u32x4 v = *(const u32x4*)(vt + d * 32 + c * 8);
          *(u32x4*)(o2 + ((size_t)head * 16 + d) * S_ + tokb + c * 8) = v;
        }
      } else {
#pragma unroll
        for (int i = 0; i < MB; ++i) {
          int tok = tokb + i * 16 + r16;
          float zz[4];
#pragma unroll
          for (int e = 0; e < 4; ++e) {
            float x = rbf(rstd[i] * (acc[i][j][e] - mean[i] * c4[e]));
            float u = 0.7978845608028654f * (x + 0.044715f * x * x * x);
            float ex = __expf(-2.f * u);
            float gl = x / (1.f + ex);
            zz[e] = rbf(gl);
            ps1[i] += zz[e];
            ps2[i] = fmaf(zz[e], zz[e], ps2[i]);
          }
          *(u32x2*)(o0 + (size_t)tok * F_ + n + 4 * g) = (u32x2){pack2(zz[0], zz[1]), pack2(zz[2], zz[3])};
        }
      }
    }
    if constexpr (MODE == M_WIN) {
      if (n0 >= FH_) {
        int p = (n0 - FH_) / BN;
#pragma unroll
        for (int i = 0; i < MB; ++i) {
          float a = ps1[i], b = ps2[i];
          a += __shfl_xor(a, 16); a += __shfl_xor(a, 32);
          b += __shfl_xor(b, 16); b += __shfl_xor(b, 32);
          int tok = tokb + i * 16 + r16;
          if (g == 0) *(f32x2*)(partout + (size_t)(p * 2048 + tok) * 2) = (f32x2){a, b};
        }
      }
    }
  } else if constexpr (MODE == M_WO || MODE == M_OUT) {
#pragma unroll
    for (int i = 0; i < MB; ++i) {
      int tok = tokb + i * 16 + r16;
#pragma unroll
      for (int j = 0; j < NB; ++j) {
        int n = nbase + j * 16 + 4 * g;
        u32x2 rres = *(const u32x2*)(e0 + (size_t)tok * D_ + n);
        float v0 = bflo(rres[0]) + rbf(acc[i][j][0]);
        float v1 = bfhi(rres[0]) + rbf(acc[i][j][1]);
        float v2 = bflo(rres[1]) + rbf(acc[i][j][2]);
        float v3 = bfhi(rres[1]) + rbf(acc[i][j][3]);
        *(u32x2*)(o0 + (size_t)tok * D_ + n) = (u32x2){pack2(v0, v1), pack2(v2, v3)};
      }
    }
  } else {
    static_assert(MB == 2 && WK == 2, "SP epilogue");
    wait_lgkm0();
    bar();
    f32x4* ex = (f32x4*)smem;
    {
#pragma unroll
      for (int j = 0; j < NB; ++j) ex[(wid * (NB + 1) + j) * 64 + lane] = wk ? acc[0][j] : acc[1][j];
      float co = wk ? cc[0] : cc[1];
      ex[(wid * (NB + 1) + NB) * 64 + lane] = (f32x4){co, 0.f, 0.f, 0.f};
    }
    wait_lgkm0();
    bar();
    const int pw = wid ^ 1;
    float c = (wk ? cc[1] : cc[0]) + ex[(pw * (NB + 1) + NB) * 64 + lane][0];
    c += __shfl_xor(c, 16);
    c += __shfl_xor(c, 32);
    const int tok = tokb + wk * 16 + r16;
    const float bias = bflo((unsigned)e1[tok]);
#pragma unroll
    for (int j = 0; j < NB; ++j) {
      f32x4 v = (wk ? acc[1][j] : acc[0][j]) + ex[(pw * (NB + 1) + j) * 64 + lane];
      int n = nbase + j * 16 + 4 * g;
      u32x2 z1 = *(const u32x2*)(e0 + (size_t)tok * F_ + n);
      float g0 = rbf(rbf(v[0] - c) + bias), g1 = rbf(rbf(v[1] - c) + bias);
      float g2 = rbf(rbf(v[2] - c) + bias), g3 = rbf(rbf(v[3] - c) + bias);
      *(u32x2*)(o0 + (size_t)tok * FH_ + n) =
          (u32x2){pack2(bflo(z1[0]) * g0, bfhi(z1[0]) * g1), pack2(bflo(z1[1]) * g2, bfhi(z1[1]) * g3)};
    }
  }
}

__global__ __launch_bounds__(256, 2) void attn_k(const u16* __restrict__ Q, const u16* __restrict__ K,
                                                 const u16* __restrict__ Vt, u16* __restrict__ O) {
  const int lane = threadIdx.x & 63;
  const int wid = __builtin_amdgcn_readfirstlane(threadIdx.x >> 6);
  const int head = blockIdx.x >> 4;
  const int qt = (blockIdx.x & 15) * 4 + wid;
  const int q0 = qt * 32;
  const int i = lane & 31, hh = lane >> 5;
  const int t0 = (q0 >= 256) ? 0 : (8 - qt);
  const int pi = ((i >> 2) & 1) * 16 + ((i >> 4) & 1) * 8 + ((i >> 3) & 1) * 4 + (i & 3);

  const u16* Qh = Q + (size_t)head * S_ * 16;
  const u16* Kh = K + (size_t)head * S_ * 16;
  const u16* Vh = Vt + (size_t)head * 16 * S_;

  u32x4 qf = *(const u32x4*)(Qh + (q0 + i) * 16 + hh * 8);
  u32x4 kf[9];
#pragma unroll
  for (int t = 0; t < 9; ++t) {
    kf[t] = (u32x4){0u, 0u, 0u, 0u};
    if (t >= t0) kf[t] = *(const u32x4*)(Kh + (q0 - 256 + 32 * t + pi) * 16 + hh * 8);
  }
  u32x4 vf[9][2];
  const unsigned fill = (i == 16) ? 0x3F803F80u : 0u;
#pragma unroll
  for (int t = 0; t < 9; ++t) {
    vf[t][0] = (u32x4){fill, fill, fill, fill};
    vf[t][1] = (u32x4){fill, fill, fill, fill};
    if (t >= t0 && i < 16) {
      const u16* p = Vh + i * S_ + (q0 - 256 + 32 * t) + 16 * hh;
      vf[t][0] = *(const u32x4*)(p);
      vf[t][1] = *(const u32x4*)(p + 8);
    }
  }

  f32x16 sc[9];
  f32x16 zero;
#pragma unroll
  for (int r = 0; r < 16; ++r) zero[r] = 0.f;
#pragma unroll
  for (int t = 0; t < 9; ++t) sc[t] = mfma32(kf[t], qf, zero);

  const float NEG = -1e30f;
#pragma unroll
  for (int r = 0; r < 16; ++r) {
    int kap = 16 * hh + r;
    if (!(kap > i)) sc[0][r] = NEG;
    if (!(kap <= i)) sc[8][r] = NEG;
  }
  float mx = NEG;
#pragma unroll
  for (int t = 0; t < 9; ++t) {
    if (t >= t0) {
#pragma unroll
      for (int r = 0; r < 16; ++r) mx = fmaxf(mx, sc[t][r]);
    }
  }
  mx = fmaxf(mx, __shfl_xor(mx, 32));
  const float cexp = 0.25f * 1.4426950408889634f;
  const float mc = mx * cexp;

  f32x16 oacc = zero;
#pragma unroll
  for (int t = 0; t < 9; ++t) {
    if (t >= t0) {
      float p[16];
#pragma unroll
      for (int r = 0; r < 16; ++r) p[r] = __builtin_amdgcn_exp2f(fmaf(sc[t][r], cexp, -mc));
      u32x4 p0 = {pack2(p[0], p[1]), pack2(p[2], p[3]), pack2(p[4], p[5]), pack2(p[6], p[7])};
      u32x4 p1 = {pack2(p[8], p[9]), pack2(p[10], p[11]), pack2(p[12], p[13]), pack2(p[14], p[15])};
      oacc = mfma32(vf[t][0], p0, oacc);
      oacc = mfma32(vf[t][1], p1, oacc);
    }
  }
  float l = __shfl(oacc[8], i);
  float inv = 1.f / l;
  u16* op = O + (size_t)(q0 + i) * D_ + head * 16 + 4 * hh;
  *(u32x2*)(op) = (u32x2){pack2(oacc[0] * inv, oacc[1] * inv), pack2(oacc[2] * inv, oacc[3] * inv)};
  *(u32x2*)(op + 8) = (u32x2){pack2(oacc[4] * inv, oacc[5] * inv), pack2(oacc[6] * inv, oacc[7] * inv)};
}

extern "C" void kernel_launch(void* const* d_in, const int* in_sizes, int n_in, void* d_out, int out_size,
                              void* d_ws, size_t ws_size, hipStream_t stream) {
  const u16* x = (const u16*)d_in[0];
  const u16* wqkv = (const u16*)d_in[1];
  const u16* wo = (const u16*)d_in[2];
  const u16* w_in = (const u16*)d_in[4];
  const u16* w_sp = (const u16*)d_in[5];
  const u16* b_sp = (const u16*)d_in[6];
  const u16* w_out = (const u16*)d_in[7];
  u16* out = (u16*)d_out;

  char* ws = (char*)d_ws;
  u16* Qb = (u16*)(ws);
  u16* Kb = (u16*)(ws + (2u << 20));
  u16* Vb = (u16*)(ws + (4u << 20));
  u16* Ob = (u16*)(ws + (6u << 20));
  u16* Hb = (u16*)(ws + (8u << 20));
  u16* Zb = (u16*)(ws + (10u << 20));
  u16* Ub = (u16*)(ws + (18u << 20));
  float* Pb = (float*)(ws + (22u << 20));

  gemm_k<128, 96, 4, 1, 1, 8, 5, M_QKV, 512, 1536, 4, 8><<<256, 256, 0, stream>>>(
      x, wqkv, nullptr, nullptr, nullptr, Qb, Kb, Vb, nullptr);
  attn_k<<<512, 256, 0, stream>>>(Qb, Kb, Vb, Ob);
  gemm_k<64, 64, 2, 2, 1, 8, 8, M_WO, 512, 512, 4, 8><<<256, 256, 0, stream>>>(
      Ob, wo, x, nullptr, nullptr, Hb, nullptr, nullptr, nullptr);
  gemm_k<128, 112, 4, 1, 1, 8, 5, M_WIN, 512, 1792, 4, 8><<<256, 256, 0, stream>>>(
      Hb, w_in, nullptr, nullptr, nullptr, Zb, nullptr, nullptr, Pb);
  gemm_k<64, 112, 2, 1, 2, 32, 6, M_SP, 2048, 1792, 8, 4><<<256, 256, 0, stream>>>(
      w_sp, Zb + FH_, Zb, b_sp, Pb, Ub, nullptr, nullptr, nullptr);
  gemm_k<64, 64, 2, 2, 1, 14, 8, M_OUT, 896, 512, 4, 8><<<256, 256, 0, stream>>>(
      Ub, w_out, Hb, nullptr, nullptr, out, nullptr, nullptr, nullptr);
}
```

```cpp
#include <hip/hip_runtime.h>
#include <hip/hip_bf16.h>
#include <type_traits>

typedef unsigned short u16;
typedef short s16x8 __attribute__((ext_vector_type(8)));
typedef short s16x4 __attribute__((ext_vector_type(4)));
typedef float f32x4 __attribute__((ext_vector_type(4)));
typedef float f32x2 __attribute__((ext_vector_type(2)));
typedef float f32x16 __attribute__((ext_vector_type(16)));
typedef __bf16 bf16x2 __attribute__((ext_vector_type(2)));
typedef __bf16 bf16x8 __attribute__((ext_vector_type(8)));
typedef unsigned u32x4 __attribute__((ext_vector_type(4)));
typedef unsigned u32x2 __attribute__((ext_vector_type(2)));

#define DI __device__ __forceinline__
#define SCHED_FENCE __builtin_amdgcn_sched_barrier(0)

constexpr int S_ = 2048, D_ = 512, H_ = 32, DH_ = 16, F_ = 1792, FH_ = 896;

DI unsigned pack2(float a, float b) {
  bf16x2 v = {(__bf16)a, (__bf16)b};
  return __builtin_bit_cast(unsigned, v);
}
DI float bflo(unsigned u) { return __uint_as_float(u << 16); }
DI float bfhi(unsigned u) { return __uint_as_float(u & 0xffff0000u); }
DI float rbf(float a) {
  __bf16 v = (__bf16)a;
  return __uint_as_float(((unsigned)__builtin_bit_cast(u16, v)) << 16);
}
DI float red4(float v) {
  unsigned u = __float_as_uint(v);
  auto r = __builtin_amdgcn_permlane16_swap(u, u, false, false);
  float a = __uint_as_float(r[0]) + __uint_as_float(r[1]);
  unsigned w = __float_as_uint(a);
  auto q = __builtin_amdgcn_permlane32_swap(w, w, false, false);
  return __uint_as_float(q[0]) + __uint_as_float(q[1]);
}
DI float rowsum16(float v) {
  v += __int_as_float(__builtin_amdgcn_update_dpp(0, __float_as_int(v), 0x111, 0xf, 0xf, true));
  v += __int_as_float(__builtin_amdgcn_update_dpp(0, __float_as_int(v), 0x112, 0xf, 0xf, true));
  v += __int_as_float(__builtin_amdgcn_update_dpp(0, __float_as_int(v), 0x114, 0xf, 0xf, true));
  v += __int_as_float(__builtin_amdgcn_update_dpp(0, __float_as_int(v), 0x118, 0xf, 0xf, true));
  return v;
}
DI float dot2(unsigned a, unsigned b, float c) {
  return __builtin_amdgcn_fdot2_f32_bf16(__builtin_bit_cast(bf16x2, a), __builtin_bit_cast(bf16x2, b), c, false);
}
DI f32x4 mfma16(u32x4 a, u32x4 b, f32x4 c) {
  return __builtin_amdgcn_mfma_f32_16x16x32_bf16(__builtin_bit_cast(bf16x8, a), __builtin_bit_cast(bf16x8, b), c, 0, 0, 0);
}
DI f32x16 mfma32(u32x4 a, u32x4 b, f32x16 c) {
  return __builtin_amdgcn_mfma_f32_32x32x16_bf16(__builtin_bit_cast(bf16x8, a), __builtin_bit_cast(bf16x8, b), c, 0, 0, 0);
}
template <int N> DI void wait_vm() { asm volatile("s_waitcnt vmcnt(%0)" ::"n"(N) : "memory"); }
DI void wait_lgkm0() { asm volatile("s_waitcnt lgkmcnt(0)" ::: "memory"); }
DI void bar() {
  asm volatile("" ::: "memory");
  __builtin_amdgcn_s_barrier();
  asm volatile("" ::: "memory");
}

template <int OFF> DI u32x2 tr_read(unsigned addr) {
  u32x2 r;
  asm volatile("ds_read_b64_tr_b16 %0, %1 offset:%2" : "=v"(r) : "v"(addr), "n"(OFF));
  return r;
}
template <int OFF> DI u32x4 lds_read128(unsigned addr) {
  u32x4 r;
  asm volatile("ds_read_b128 %0, %1 offset:%2" : "=v"(r) : "v"(addr), "n"(OFF));
  return r;
}
template <int N> DI void wait_lgkm() { asm volatile("s_waitcnt lgkmcnt(%0)" ::"n"(N) : "memory"); }
template <int I, int N, class F> DI void static_for(F&& f) {
  if constexpr (I < N) {
    f(std::integral_constant<int, I>{});
    static_for<I + 1, N>(f);
  }
}
DI void glds16(const void* g, void* l) {
  __builtin_amdgcn_global_load_lds((const unsigned*)g, (unsigned*)l, 16, 0, 0);
}

template <int KT, int NS> constexpr int iss_before(int kt) {
  int is = 2;
  for (int k = 0; k < kt; ++k) {
    int n = is + 2;
    if (n > k + NS) n = k + NS;
    if (n > KT) n = KT;
    is = n;
  }
  return is;
}
enum { M_QKV = 0, M_WO = 1, M_WIN = 2, M_SP = 3, M_OUT = 4 };

template <int BM, int BN, int WM, int WN, int WK, int SUB, int KT, int NS, int MODE, int LDA, int LDB, int PM, int PN>
__global__ __launch_bounds__(768, 3) void gemm_k(const u16* __restrict__ A, const u16* __restrict__ B,
                                                 const u16* __restrict__ e0, const u16* __restrict__ e1,
                                                 const float* __restrict__ partin, u16* __restrict__ o0,
                                                 u16* __restrict__ o1, u16* __restrict__ o2,
                                                 float* __restrict__ partout) {
  static_assert(WM * WN * WK == 8, "8 consumer waves");
  constexpr int AB = BM * 128, BB = BN * 128, SUBB = AB + BB, STG = SUB * SUBB;
  constexpr int AI = AB / 1024, BI = BB / 1024;
  static_assert(AI % 4 == 0, "A instr");
  constexpr int LA = AI / 4;
  constexpr int LBF = BI / 4, LBR = BI % 4;
  constexpr int MB = BM / WM / 16, NB = BN / WN / 16;
  constexpr int RING = NS * STG;
  constexpr int EXTRA = (MODE == M_SP) ? 16384 : 0;
  constexpr bool LNA = (MODE == M_QKV || MODE == M_WIN);
  __shared__ __attribute__((aligned(16))) char smem[RING + EXTRA];

  const int tid = threadIdx.x, lane = tid & 63;
  const int wid = __builtin_amdgcn_readfirstlane(tid >> 6);

  constexpr int TN = (MODE == M_QKV ? 1536 : MODE == M_WIN ? 1792 : MODE == M_SP ? 896 : 512) / BN;
  constexpr int PXN = TN / PN;
  const int bid = blockIdx.x, xcd = bid & 7, jj = bid >> 3;
  const int tm = (xcd / PXN) * PM + jj / PN, tn = (xcd % PXN) * PN + jj % PN;
  const int m0 = tm * BM, n0 = tn * BN;

  if (wid >= 8) {
    const int lw = wid - 8;
    unsigned aoff[LA], boff[LBF + 1];
#pragma unroll
    for (int i = 0; i < LA; ++i) {
      int q = lw + 4 * i;
      int row = q * 8 + (lane >> 3), pos = lane & 7;
      int ch = pos ^ (row & 7);
      if constexpr (MODE == M_WO) aoff[i] = (unsigned)(((ch >> 2) * S_ + m0 + row) * 32 + (ch & 3) * 8);
      else aoff[i] = (unsigned)((m0 + row) * LDA + (ch << 3));
    }
#pragma unroll
    for (int i = 0; i < LBF; ++i) {
      int q = lw + 4 * i;
      int r = (q & 7) * 8 + (lane >> 3), pos = lane & 7;
      int h = ((r >> 1) & 1) | (((r >> 3) & 1) << 1);
      int ch = (((pos >> 1) ^ h) << 1) | (pos & 1);
      boff[i] = (unsigned)(r * LDB + n0 + (q >> 3) * 64 + ch * 8);
    }
    static_assert(LBR == 0, "BN % 32");
    const bool extraB = (lw < LBR);
    auto issue = [&](int kt, int slot) {
#pragma unroll
      for (int sb = 0; sb < SUB; ++sb) {
        const u16* a = A + (kt * SUB + sb) * (MODE == M_WO ? 2 * S_ * 32 : 64);
        const u16* b = B + (size_t)(kt * SUB + sb) * 64 * LDB;
        char* s = smem + slot * STG + sb * SUBB;
#pragma unroll
        for (int i = 0; i < LA; ++i) glds16(a + aoff[i], s + (lw + 4 * i) * 1024);
#pragma unroll
        for (int i = 0; i < LBF; ++i) glds16(b + boff[i], s + AB + (lw + 4 * i) * 1024);
        if (LBR != 0) {
          if (extraB) glds16(b + boff[LBF], s + AB + (lw + 4 * LBF) * 1024);
        }
      }
    };
    issue(0, 0);
    issue(1, 1);
    static_for<0, KT>([&](auto KTI) {
      constexpr int kt = KTI;
      constexpr int is0 = iss_before<KT, NS>(kt);
      constexpr int is1 = iss_before<KT, NS>(kt + 1);
      constexpr int later = is0 - 1 - kt;
      if (LBR != 0 && extraB) wait_vm<later * SUB * (LA + LBF + 1)>();
      else wait_vm<later * SUB * (LA + LBF)>();
      bar();
      if constexpr (is1 > is0) issue(is0, is0 % NS);
      if constexpr (is1 > is0 + 1) issue(is0 + 1, (is0 + 1) % NS);
    });
    return;
  }

  const int wk = wid % WK;
  const int wmn = wid / WK;
  const int wm = wmn / WN, wn = wmn % WN;
  const int r16 = lane & 15, g = lane >> 4;
  const int tokb = m0 + wm * (BM / WM);
  const int nbase = n0 + wn * (BN / WN);

  if constexpr (MODE == M_SP) {
    float* sr = (float*)(smem + RING);
    float* srm = sr + 2048;
#pragma unroll
    for (int j = 0; j < 2; ++j) {
      int row = 2 * tid + 1024 * j;
      f32x4 t = {0.f, 0.f, 0.f, 0.f};
#pragma unroll
      for (int p = 0; p < 7; ++p) {
        f32x4 v = *(const f32x4*)(partin + (size_t)(p * 2048 + row) * 2);
        t += v;
      }
      float mean0 = t[0] * (1.f / 896.f), mean1 = t[2] * (1.f / 896.f);
      float var0 = fmaxf(t[1] * (1.f / 896.f) - mean0 * mean0, 0.f);
      float var1 = fmaxf(t[3] * (1.f / 896.f) - mean1 * mean1, 0.f);
      float r0 = rsqrtf(var0 + 1e-5f), r1 = rsqrtf(var1 + 1e-5f);
      *(f32x2*)(sr + row) = (f32x2){r0, r1};
      *(f32x2*)(srm + row) = (f32x2){r0 * mean0, r1 * mean1};
    }
  }

  f32x4 acc[MB][NB];
#pragma unroll
  for (int i = 0; i < MB; ++i)
#pragma unroll
    for (int j = 0; j < NB; ++j) acc[i][j] = (f32x4){0.f, 0.f, 0.f, 0.f};
  float s1[MB], s2[MB], cs[NB];
  f32x2 cc2[MB];
#pragma unroll
  for (int i = 0; i < MB; ++i) { s1[i] = 0.f; s2[i] = 0.f; cc2[i] = (f32x2){0.f, 0.f}; }
#pragma unroll
  for (int j = 0; j < NB; ++j) cs[j] = 0.f;

  constexpr int NKS = 2 * SUB / WK;
  static_assert(NKS >= 1 && NKS <= 2, "NKS");
  static_assert(NKS == 1 || WK == 1, "NKS");
  const unsigned lbase = (unsigned)(size_t)smem;
  unsigned ard[NKS], bpo[4];
  {
    int q = (lane & 15) >> 2, p = lane & 3;
    int row = wm * (BM / WM) + r16;
#pragma unroll
    for (int kq = 0; kq < NKS; ++kq) {
      int kk = wk + WK * kq;
      int sub = kk >> 1, ks = kk & 1;
      ard[kq] = lbase + sub * SUBB + row * 128 + (((4 * ks + g) ^ (row & 7)) << 4);
    }
    int h = ((q >> 1) & 1) | ((g & 1) << 1);
    int kk0 = wk;
    unsigned bb = lbase + (kk0 >> 1) * SUBB + AB + ((kk0 & 1) * 32 + 8 * g + q) * 128 + p * 8;
    constexpr int NBW = BN / WN / 16;
    static_assert(NBW % 4 == 0 || NBW == 2, "wave n-blocks");
    int nb0 = wn * NBW;
#pragma unroll
    for (int pc = 0; pc < 4; ++pc) {
      int nb = nb0 + pc;
      bpo[pc] = bb + (nb >> 2) * 8192 + (((nb & 3) ^ h) << 5);
    }
  }
  unsigned strd = lbase + RING + ((wk & 1) * 32 + (wk >> 1) * 64 + g * 8) * 4;

  constexpr int ST = (MODE == M_SP) ? 4 : 0;
  constexpr int PER = ST + MB + 2 * NB;
  constexpr int RTOT = NKS * PER;
  constexpr int NB0 = (NB + 1) / 2;

  int slot = 0;
#pragma unroll 1
  for (int kt = 0; kt < KT; ++kt) {
    wait_lgkm0();
    bar();
    const unsigned so = slot * STG;
    u32x4 af[NKS][MB];
    u32x2 bl[NKS][NB], bh[NKS][NB];
    u32x4 st[4];
    static_for<0, NKS>([&](auto KQ) {
      if constexpr (MODE == M_SP) {
        st[0] = lds_read128<0>(strd);
        st[1] = lds_read128<16>(strd);
        st[2] = lds_read128<8192>(strd);
        st[3] = lds_read128<8208>(strd);
      }
      static_for<0, MB>([&](auto I) { af[KQ][I] = lds_read128<I * 2048>(ard[KQ] + so); });
      static_for<0, NB>([&](auto J) {
        constexpr int off = KQ * 4096 + (J >> 2) * 8192;
        bl[KQ][J] = tr_read<off>(bpo[J & 3] + so);
        bh[KQ][J] = tr_read<off + 512>(bpo[J & 3] + so);
      });
    });
    if constexpr (MODE == M_SP) strd += SUB * 256;
    const bool docs = LNA && ((kt % WM) == wm);
    static_for<0, NKS>([&](auto KQ) {
      constexpr int w0 = RTOT - (KQ * PER + ST + MB + 2 * NB0);
      constexpr int w1 = RTOT - (KQ + 1) * PER;
      constexpr int wa = RTOT - (KQ * PER + ST + MB);
      wait_lgkm<(wa > 15 ? 15 : wa)>();
      u32x4 a[MB];
#pragma unroll
      for (int i = 0; i < MB; ++i) {
        asm volatile("" : "+v"(af[KQ][i]));
        a[i] = af[KQ][i];
      }
      if constexpr (MODE == M_SP) {
        asm volatile("" : "+v"(st[0]), "+v"(st[1]), "+v"(st[2]), "+v"(st[3]));
        f32x4 r0 = __builtin_bit_cast(f32x4, st[0]), r1 = __builtin_bit_cast(f32x4, st[1]);
        f32x4 m0v = __builtin_bit_cast(f32x4, st[2]), m1v = __builtin_bit_cast(f32x4, st[3]);
#pragma unroll
        for (int i = 0; i < MB; ++i) {
          f32x2 w01 = {bflo(a[i][0]), bfhi(a[i][0])}, w23 = {bflo(a[i][1]), bfhi(a[i][1])};
          f32x2 w45 = {bflo(a[i][2]), bfhi(a[i][2])}, w67 = {bflo(a[i][3]), bfhi(a[i][3])};
          f32x2 c = cc2[i];
          c = __builtin_elementwise_fma(w01, (f32x2){m0v[0], m0v[1]}, c);
          c = __builtin_elementwise_fma(w23, (f32x2){m0v[2], m0v[3]}, c);
          c = __builtin_elementwise_fma(w45, (f32x2){m1v[0], m1v[1]}, c);
          c = __builtin_elementwise_fma(w67, (f32x2){m1v[2], m1v[3]}, c);
          cc2[i] = c;
          w01 *= (f32x2){r0[0], r0[1]}; w23 *= (f32x2){r0[2], r0[3]};
          w45 *= (f32x2){r1[0], r1[1]}; w67 *= (f32x2){r1[2], r1[3]};
          a[i][0] = pack2(w01[0], w01[1]);
          a[i][1] = pack2(w23[0], w23[1]);
          a[i][2] = pack2(w45[0], w45[1]);
          a[i][3] = pack2(w67[0], w67[1]);
        }
      }
      if constexpr (LNA) {
#pragma unroll
        for (int i = 0; i < MB; ++i) {
          if constexpr (WN == 2) {
#pragma unroll
            for (int e = 0; e < 4; ++e) s1[i] = dot2(a[i][e], wn ? a[i][e] : 0x3F803F80u, s1[i]);
          } else {
#pragma unroll
            for (int e = 0; e < 4; ++e) {
              s1[i] = dot2(a[i][e], 0x3F803F80u, s1[i]);
              s2[i] = dot2(a[i][e], a[i][e], s2[i]);
            }
          }
        }
      }
#pragma unroll
      for (int i = 0; i < MB; ++i) asm volatile("" : "+v"(a[i]));
      wait_lgkm<(w0 > 15 ? 15 : w0)>();
#pragma unroll
      for (int j = 0; j < NB0; ++j) {
        asm volatile("" : "+v"(bl[KQ][j]), "+v"(bh[KQ][j]));
        u32x4 b = {bl[KQ][j][0], bl[KQ][j][1], bh[KQ][j][0], bh[KQ][j][1]};
#pragma unroll
        for (int i = 0; i < MB; ++i) acc[i][j] = mfma16(b, a[i], acc[i][j]);
      }
      wait_lgkm<(w1 > 15 ? 15 : w1)>();
#pragma unroll
      for (int j = NB0; j < NB; ++j) {
        asm volatile("" : "+v"(bl[KQ][j]), "+v"(bh[KQ][j]));
        u32x4 b = {bl[KQ][j][0], bl[KQ][j][1], bh[KQ][j][0], bh[KQ][j][1]};
#pragma unroll
        for (int i = 0; i < MB; ++i) acc[i][j] = mfma16(b, a[i], acc[i][j]);
      }
      if constexpr (LNA) {
        if (docs) {
#pragma unroll
          for (int j = 0; j < NB; ++j) {
            cs[j] = dot2(bl[KQ][j][0], 0x3F803F80u, cs[j]);
            cs[j] = dot2(bl[KQ][j][1], 0x3F803F80u, cs[j]);
            cs[j] = dot2(bh[KQ][j][0], 0x3F803F80u, cs[j]);
            cs[j] = dot2(bh[KQ][j][1], 0x3F803F80u, cs[j]);
          }
        }
      }
    });
    ++slot; if (slot == NS) slot = 0;
  }

  if constexpr (MODE != M_QKV) {
    constexpr int RSF = BN * 4 + 16;
    constexpr int PLB = BM * RSF;
    constexpr int O_ROWP = WK * PLB;
    constexpr int O_COLP = O_ROWP + WK * BM * 8;
    constexpr int O_ROWF = O_COLP + 8 * BN * 4;
    constexpr int O_COLF = O_ROWF + BM * 8;
    static_assert(O_COLF + BN * 4 <= RING, "epilogue LDS");
    constexpr int CH = BN / 8;
    constexpr int LPR = (CH <= 8) ? 8 : 16;
    constexpr int RPP = 512 / LPR;
    constexpr int NPASS = BM / RPP;
    const int ec = tid & (LPR - 1), er = tid / LPR;
    const bool eact = ec < CH;
    u32x4 eop[NPASS];
    if constexpr (MODE != M_WIN) {
      constexpr int LDE = (MODE == M_SP) ? F_ : D_;
#pragma unroll
      for (int ps = 0; ps < NPASS; ++ps)
        if (eact) eop[ps] = *(const u32x4*)(e0 + (size_t)(m0 + er + ps * RPP) * LDE + n0 + ec * 8);
    }
    wait_lgkm0();
    bar();
    {
      char* pl = smem + wk * PLB + (wm * (BM / WM) + r16) * RSF + (wn * (BN / WN) + 4 * g) * 4;
#pragma unroll
      for (int i = 0; i < MB; ++i)
#pragma unroll
        for (int j = 0; j < NB; ++j) *(f32x4*)(pl + i * 16 * RSF + j * 64) = acc[i][j];
    }
    if constexpr (MODE == M_WIN || MODE == M_SP) {
#pragma unroll
      for (int i = 0; i < MB; ++i) {
        if constexpr (MODE == M_WIN && WN == 2) {
          float a = red4(s1[i]);
          if (g == 0) *(float*)(smem + O_ROWP + (wk * BM + wm * (BM / WM) + i * 16 + r16) * 8 + wn * 4) = a;
        } else {
          float a = (MODE == M_WIN) ? red4(s1[i]) : red4(cc2[i][0] + cc2[i][1]);
          float b = (MODE == M_WIN) ? red4(s2[i]) : 0.f;
          if (g == 0) *(f32x2*)(smem + O_ROWP + (wk * BM + wm * (BM / WM) + i * 16 + r16) * 8) = (f32x2){a, b};
        }
      }
    }
    if constexpr (MODE == M_WIN) {
#pragma unroll
      for (int j = 0; j < NB; ++j) {
        float v = red4(cs[j]);
        if (g == 0) *(float*)(smem + O_COLP + ((wm * WK + wk) * BN + wn * (BN / WN) + j * 16 + r16) * 4) = v;
      }
    }
    wait_lgkm0();
    bar();
    if constexpr (MODE == M_WIN) {
      if (tid < BN) {
        float c = 0.f;
#pragma unroll
        for (int w = 0; w < WM * WK; ++w) c += *(const float*)(smem + O_COLP + (w * BN + tid) * 4);
        *(float*)(smem + O_COLF + tid * 4) = c;
      } else if (tid >= 128 && tid < 128 + BM) {
        int row = tid - 128;
        f32x2 t = {0.f, 0.f};
#pragma unroll
        for (int k = 0; k < WK; ++k) t += *(const f32x2*)(smem + O_ROWP + (k * BM + row) * 8);
        float mean = t[0] * (1.f / 512.f);
        float var = fmaxf(t[1] * (1.f / 512.f) - mean * mean, 0.f);
        *(f32x2*)(smem + O_ROWF + row * 8) = (f32x2){mean, rsqrtf(var + 1e-5f)};
      }
      wait_lgkm0();
      bar();
    }
    f32x4 cf0 = {0.f, 0.f, 0.f, 0.f}, cf1 = cf0;
    if constexpr (MODE == M_WIN) {
      if (eact) {
        cf0 = *(const f32x4*)(smem + O_COLF + ec * 32);
        cf1 = *(const f32x4*)(smem + O_COLF + ec * 32 + 16);
      }
    }
#pragma unroll
    for (int ps = 0; ps < NPASS; ++ps) {
      const int row = er + ps * RPP;
      const int tok = m0 + row;
      float st1 = 0.f, st2 = 0.f;
      if (eact) {
        f32x4 v0 = *(const f32x4*)(smem + row * RSF + ec * 32);
        f32x4 v1 = *(const f32x4*)(smem + row * RSF + ec * 32 + 16);
#pragma unroll
        for (int k = 1; k < WK; ++k) {
          v0 += *(const f32x4*)(smem + k * PLB + row * RSF + ec * 32);
          v1 += *(const f32x4*)(smem + k * PLB + row * RSF + ec * 32 + 16);
        }
        float v[8] = {v0[0], v0[1], v0[2], v0[3], v1[0], v1[1], v1[2], v1[3]};
        u32x4 ov;
        if constexpr (MODE == M_WO || MODE == M_OUT) {
#pragma unroll
          for (int e = 0; e < 4; ++e)
            ov[e] = pack2(bflo(eop[ps][e]) + rbf(v[2 * e]), bfhi(eop[ps][e]) + rbf(v[2 * e + 1]));
          *(u32x4*)(o0 + (size_t)tok * D_ + n0 + ec * 8) = ov;
        } else if constexpr (MODE == M_SP) {
          float c = 0.f;
#pragma unroll
          for (int k = 0; k < WK; ++k) c += *(const float*)(smem + O_ROWP + (k * BM + row) * 8);
          const float bias = bflo((unsigned)e1[tok]);
#pragma unroll
          for (int e = 0; e < 4; ++e) {
            float g0 = rbf(rbf(v[2 * e] - c) + bias), g1 = rbf(rbf(v[2 * e + 1] - c) + bias);
            ov[e] = pack2(bflo(eop[ps][e]) * g0, bfhi(eop[ps][e]) * g1);
          }
          *(u32x4*)(o0 + (size_t)tok * FH_ + n0 + ec * 8) = ov;
        } else {
          f32x2 ms = *(const f32x2*)(smem + O_ROWF + row * 8);
          float cf[8] = {cf0[0], cf0[1], cf0[2], cf0[3], cf1[0], cf1[1], cf1[2], cf1[3]};
          float zz[8];
#pragma unroll
          for (int e = 0; e < 8; ++e) {
            float x = rbf(ms[1] * (v[e] - ms[0] * cf[e]));
            float a = x * fmaf(x * x, -0.10294324f, -2.3022082f);
            float t = __builtin_amdgcn_exp2f(a);
            zz[e] = x * __builtin_amdgcn_rcpf(1.f + t);
          }
#pragma unroll
          for (int e = 0; e < 4; ++e) ov[e] = pack2(zz[2 * e], zz[2 * e + 1]);
          *(u32x4*)(o0 + (size_t)tok * F_ + n0 + ec * 8) = ov;
          if (n0 >= FH_) {
#pragma unroll
            for (int e = 0; e < 4; ++e) {
              st1 = dot2(ov[e], 0x3F803F80u, st1);
              st2 = dot2(ov[e], ov[e], st2);
            }
          }
        }
      }
      if constexpr (MODE == M_WIN) {
        if (n0 >= FH_) {
          static_assert(LPR == 16, "row reduce");
          st1 = rowsum16(st1);
          st2 = rowsum16(st2);
          if (ec == 15) *(f32x2*)(partout + (size_t)(((n0 - FH_) / BN) * 2048 + tok) * 2) = (f32x2){st1, st2};
        }
      }
    }
    return;
  }
  if constexpr (MODE == M_QKV) {
    static_assert(WK == 1 && WN == 2 && MB == 2, "qkv epilogue");
    wait_lgkm0();
    bar();
    float* csl = (float*)smem;
    f32x4* ex = (f32x4*)(smem + 4096);
#pragma unroll
    for (int j = 0; j < NB; ++j) {
      float v = red4(cs[j]);
      if (g == 0) csl[wid * (NB * 16) + j * 16 + r16] = v;
    }
    ex[wid * 64 + lane] = (f32x4){s1[0], 0.f, s1[1], 0.f};
    wait_lgkm0();
    bar();
    {
      f32x4 o = ex[(wid ^ 1) * 64 + lane];
      float m0_ = wn ? o[0] : s1[0], q0_ = wn ? s1[0] : o[0];
      float m1_ = wn ? o[2] : s1[1], q1_ = wn ? s1[1] : o[2];
      s1[0] = m0_; s2[0] = q0_;
      s1[1] = m1_; s2[1] = q1_;
    }
    float mean[MB], rstd[MB];
#pragma unroll
    for (int i = 0; i < MB; ++i) {
      float a = red4(s1[i]), b = red4(s2[i]);
      mean[i] = a * (1.f / 512.f);
      float var = fmaxf(b * (1.f / 512.f) - mean[i] * mean[i], 0.f);
      rstd[i] = rsqrtf(var + 1e-5f);
    }
    const int which = n0 >> 9;
#pragma unroll
    for (int j = 0; j < NB; ++j) {
      f32x4 c4 = {0.f, 0.f, 0.f, 0.f};
#pragma unroll
      for (int w = 0; w < 4; ++w) c4 += *(const f32x4*)(csl + (w * 2 + wn) * (NB * 16) + j * 16 + 4 * g);
      const int n = nbase + j * 16;
      const int head = (n & 511) >> 4;
      if (which < 2) {
        u16* dst = (which == 0 ? o0 : o1);
        const float sc = (which == 0) ? 0.36067376022224085f : 1.f;
#pragma unroll
        for (int i = 0; i < MB; ++i) {
          int tok = tokb + i * 16 + r16;
          float rs = rstd[i] * sc;
          float v0 = rs * (acc[i][j][0] - mean[i] * c4[0]);
          float v1 = rs * (acc[i][j][1] - mean[i] * c4[1]);
          float v2 = rs * (acc[i][j][2] - mean[i] * c4[2]);
          float v3 = rs * (acc[i][j][3] - mean[i] * c4[3]);
          *(u32x2*)(dst + ((size_t)head * S_ + tok) * 16 + 4 * g) = (u32x2){pack2(v0, v1), pack2(v2, v3)};
        }
      } else {
        u16* vt = (u16*)(smem + 16384 + (wid * NB + j) * (MB * 512));
#pragma unroll
        for (int i = 0; i < MB; ++i) {
#pragma unroll
          for (int e = 0; e < 4; ++e) {
            float v = rstd[i] * (acc[i][j][e] - mean[i] * c4[e]);
            __bf16 bv = (__bf16)v;
            vt[(4 * g + e) * (MB * 16) + i * 16 + r16] = __builtin_bit_cast(u16, bv);
          }
        }
        int d = lane >> 2, c = lane & 3;
        u32x4 v = *(const u32x4*)(vt + d * 32 + c * 8);
        *(u32x4*)(o2 + ((size_t)head * 16 + d) * S_ + tokb + c * 8) = v;
      }
    }
  }
}

constexpr int AT_KB = 384 * 32;
constexpr int AT_VS = 784;
constexpr int AT_VB = 32 * AT_VS;
constexpr int AT_HB = AT_KB + AT_VB;
__global__ __launch_bounds__(512, 2) void attn_k(const u16* __restrict__ Q, const u16* __restrict__ K,
                                                 const u16* __restrict__ Vt, u16* __restrict__ O2) {
  __shared__ __attribute__((aligned(16))) char smem[2 * AT_HB];
  const int tid = threadIdx.x, lane = tid & 63;
  const int wid = __builtin_amdgcn_readfirstlane(tid >> 6);
  const int hp = blockIdx.x >> 4, qb = blockIdx.x & 15;
  const int hl = wid >> 2;
  const int head = hp * 2 + hl;
  const int qt = qb * 4 + (wid & 3);
  const int q0 = qt * 32;
  const int kw0 = qb * 128 - 256;
  const int i = lane & 31, hh = lane >> 5;

  {
    const int t = tid & 255;
    const u16* Kh = K + (size_t)head * S_ * 16;
    const u16* Vh = Vt + (size_t)head * 16 * S_;
    char* kl = smem + hl * AT_HB;
    char* vl = kl + AT_KB;
    u32x4 kv[3], vv[3];
#pragma unroll
    for (int r = 0; r < 3; ++r) {
      int c = t + 256 * r;
      int key = kw0 + (c >> 1); key = key < 0 ? 0 : key;
      kv[r] = *(const u32x4*)(Kh + key * 16 + (c & 1) * 8);
      int d = c / 48, cc = c % 48;
      int k2 = kw0 + cc * 8; k2 = k2 < 0 ? 0 : k2;
      vv[r] = *(const u32x4*)(Vh + d * S_ + k2);
    }
#pragma unroll
    for (int r = 0; r < 3; ++r) {
      int c = t + 256 * r;
      int d = c / 48, cc = c % 48;
      unsigned f = (d == 0) ? 0x3F803F80u : 0u;
      *(u32x4*)(vl + (16 + d) * AT_VS + cc * 16) = (u32x4){f, f, f, f};
    }
#pragma unroll
    for (int r = 0; r < 3; ++r) {
      int c = t + 256 * r;
      int row = c >> 1;
      *(u32x4*)(kl + row * 32 + (((c & 1) ^ ((row >> 3) & 1)) << 4)) = kv[r];
      int d = c / 48, cc = c % 48;
      *(u32x4*)(vl + d * AT_VS + cc * 16) = vv[r];
    }
  }
  u32x4 qf = *(const u32x4*)(Q + ((size_t)head * S_ + q0 + i) * 16 + hh * 8);

  const int t0 = (q0 >= 256) ? 0 : (8 - qt);
  const int pi = ((i >> 2) & 1) * 16 + ((i >> 4) & 1) * 8 + ((i >> 3) & 1) * 4 + (i & 3);
  const float NEG = -1e30f;
  f32x16 c0, c8, zero;
#pragma unroll
  for (int r = 0; r < 16; ++r) {
    int kap = 16 * hh + r;
    zero[r] = 0.f;
    c0[r] = (kap > i) ? 0.f : NEG;
    c8[r] = (kap <= i) ? 0.f : NEG;
  }
  __syncthreads();

  const int wrow = (wid & 3) * 32;
  const char* kr = smem + hl * AT_HB + (wrow + pi) * 32 + ((hh ^ ((pi >> 3) & 1)) << 4);
  const char* vr = smem + hl * AT_HB + AT_KB + i * AT_VS + (wrow + 16 * hh) * 2;

  u32x4 kf[9];
#pragma unroll
  for (int t = 0; t < 9; ++t) kf[t] = *(const u32x4*)(kr + t * 1024);

  float mx = NEG;
#pragma unroll
  for (int t = 0; t < 9; ++t) {
    if (t >= t0) {
      f32x16 s = mfma32(kf[t], qf, t == 0 ? c0 : (t == 8 ? c8 : zero));
#pragma unroll
      for (int r = 0; r < 16; r += 2) mx = fmaxf(fmaxf(mx, s[r]), s[r + 1]);
    }
  }
  {
    unsigned u = __float_as_uint(mx);
    auto r = __builtin_amdgcn_permlane32_swap(u, u, false, false);
    mx = fmaxf(__uint_as_float(r[0]), __uint_as_float(r[1]));
  }
  f32x16 cm;
#pragma unroll
  for (int r = 0; r < 16; ++r) { cm[r] = -mx; c0[r] -= mx; c8[r] -= mx; }

  f32x16 oacc = zero;
#pragma unroll
  for (int t = 0; t < 9; ++t) {
    if (t >= t0) {
      u32x4 v0 = *(const u32x4*)(vr + t * 64);
      u32x4 v1 = *(const u32x4*)(vr + t * 64 + 16);
      f32x16 s = mfma32(kf[t], qf, t == 0 ? c0 : (t == 8 ? c8 : cm));
      float p[16];
#pragma unroll
      for (int r = 0; r < 16; ++r) p[r] = __builtin_amdgcn_exp2f(s[r]);
      u32x4 p0 = {pack2(p[0], p[1]), pack2(p[2], p[3]), pack2(p[4], p[5]), pack2(p[6], p[7])};
      u32x4 p1 = {pack2(p[8], p[9]), pack2(p[10], p[11]), pack2(p[12], p[13]), pack2(p[14], p[15])};
      oacc = mfma32(v0, p0, oacc);
      oacc = mfma32(v1, p1, oacc);
    }
  }
  float l;
  {
    unsigned u = __float_as_uint(oacc[8]);
    auto r = __builtin_amdgcn_permlane32_swap(u, u, false, false);
    l = __uint_as_float(r[0]) + __uint_as_float(r[1]);
  }
  float inv = 1.f / l;
  __syncthreads();
  {
    char* ot = smem + ((wid & 3) * 32 + i) * 64 + hl * 32 + hh * 8;
    *(u32x2*)(ot) = (u32x2){pack2(oacc[0] * inv, oacc[1] * inv), pack2(oacc[2] * inv, oacc[3] * inv)};
    *(u32x2*)(ot + 16) = (u32x2){pack2(oacc[4] * inv, oacc[5] * inv), pack2(oacc[6] * inv, oacc[7] * inv)};
  }
  __syncthreads();
  *(u32x4*)(O2 + ((size_t)hp * S_ + qb * 128) * 32 + tid * 8) = *(const u32x4*)(smem + tid * 16);
}

extern "C" void kernel_launch(void* const* d_in, const int* in_sizes, int n_in, void* d_out, int out_size,
                              void* d_ws, size_t ws_size, hipStream_t stream) {
  const u16* x = (const u16*)d_in[0];
  const u16* wqkv = (const u16*)d_in[1];
  const u16* wo = (const u16*)d_in[2];
  const u16* w_in = (const u16*)d_in[4];
  const u16* w_sp = (const u16*)d_in[5];
  const u16* b_sp = (const u16*)d_in[6];
  const u16* w_out = (const u16*)d_in[7];
  u16* out = (u16*)d_out;

  char* ws = (char*)d_ws;
  u16* Qb = (u16*)(ws);
  u16* Kb = (u16*)(ws + (2u << 20));
  u16* Vb = (u16*)(ws + (4u << 20));
  u16* Ob = (u16*)(ws + (6u << 20));
  u16* Hb = (u16*)(ws + (8u << 20));
  u16* Zb = (u16*)(ws + (10u << 20));
  u16* Ub = (u16*)(ws + (18u << 20));
  float* Pb = (float*)(ws + (22u << 20));

  gemm_k<128, 128, 4, 2, 1, 1, 8, 4, M_QKV, 512, 1536, 4, 6><<<192, 768, 0, stream>>>(
      x, wqkv, nullptr, nullptr, nullptr, Qb, Kb, Vb, nullptr);
  attn_k<<<256, 512, 0, stream>>>(Qb, Kb, Vb, Ob);
  gemm_k<64, 64, 4, 2, 1, 1, 8, 8, M_WO, 512, 512, 4, 8><<<256, 768, 0, stream>>>(
      Ob, wo, x, nullptr, nullptr, Hb, nullptr, nullptr, nullptr);
  gemm_k<128, 128, 4, 2, 1, 1, 8, 4, M_WIN, 512, 1792, 4, 7><<<224, 768, 0, stream>>>(
      Hb, w_in, nullptr, nullptr, nullptr, Zb, nullptr, nullptr, Pb);
  gemm_k<64, 128, 2, 1, 4, 2, 16, 3, M_SP, 2048, 1792, 4, 7><<<224, 768, 0, stream>>>(
      w_sp, Zb + FH_, Zb, b_sp, Pb, Ub, nullptr, nullptr, nullptr);
  gemm_k<64, 64, 4, 2, 1, 1, 14, 8, M_OUT, 896, 512, 4, 8><<<256, 768, 0, stream>>>(
      Ub, w_out, Hb, nullptr, nullptr, out, nullptr, nullptr, nullptr);
}
```

```cpp
#include <hip/hip_runtime.h>
#include <hip/hip_bf16.h>
#include <type_traits>

typedef unsigned short u16;
typedef short s16x8 __attribute__((ext_vector_type(8)));
typedef short s16x4 __attribute__((ext_vector_type(4)));
typedef float f32x4 __attribute__((ext_vector_type(4)));
typedef float f32x2 __attribute__((ext_vector_type(2)));
typedef float f32x16 __attribute__((ext_vector_type(16)));
typedef __bf16 bf16x2 __attribute__((ext_vector_type(2)));
typedef __bf16 bf16x8 __attribute__((ext_vector_type(8)));
typedef unsigned u32x4 __attribute__((ext_vector_type(4)));
typedef unsigned u32x2 __attribute__((ext_vector_type(2)));

#define DI __device__ __forceinline__
#define SCHED_FENCE __builtin_amdgcn_sched_barrier(0)

constexpr int S_ = 2048, D_ = 512, H_ = 32, DH_ = 16, F_ = 1792, FH_ = 896;

DI unsigned pack2(float a, float b) {
  bf16x2 v = {(__bf16)a, (__bf16)b};
  return __builtin_bit_cast(unsigned, v);
}
DI float bflo(unsigned u) { return __uint_as_float(u << 16); }
DI float bfhi(unsigned u) { return __uint_as_float(u & 0xffff0000u); }
DI float rbf(float a) {
  __bf16 v = (__bf16)a;
  return __uint_as_float(((unsigned)__builtin_bit_cast(u16, v)) << 16);
}
DI float red4(float v) {
  unsigned u = __float_as_uint(v);
  auto r = __builtin_amdgcn_permlane16_swap(u, u, false, false);
  float a = __uint_as_float(r[0]) + __uint_as_float(r[1]);
  unsigned w = __float_as_uint(a);
  auto q = __builtin_amdgcn_permlane32_swap(w, w, false, false);
  return __uint_as_float(q[0]) + __uint_as_float(q[1]);
}
DI float rowsum16(float v) {
  v += __int_as_float(__builtin_amdgcn_update_dpp(0, __float_as_int(v), 0x111, 0xf, 0xf, true));
  v += __int_as_float(__builtin_amdgcn_update_dpp(0, __float_as_int(v), 0x112, 0xf, 0xf, true));
  v += __int_as_float(__builtin_amdgcn_update_dpp(0, __float_as_int(v), 0x114, 0xf, 0xf, true));
  v += __int_as_float(__builtin_amdgcn_update_dpp(0, __float_as_int(v), 0x118, 0xf, 0xf, true));
  return v;
}
DI float dot2(unsigned a, unsigned b, float c) {
  return __builtin_amdgcn_fdot2_f32_bf16(__builtin_bit_cast(bf16x2, a), __builtin_bit_cast(bf16x2, b), c, false);
}
DI f32x4 mfma16(u32x4 a, u32x4 b, f32x4 c) {
  return __builtin_amdgcn_mfma_f32_16x16x32_bf16(__builtin_bit_cast(bf16x8, a), __builtin_bit_cast(bf16x8, b), c, 0, 0, 0);
}
DI f32x16 mfma32(u32x4 a, u32x4 b, f32x16 c) {
  return __builtin_amdgcn_mfma_f32_32x32x16_bf16(__builtin_bit_cast(bf16x8, a), __builtin_bit_cast(bf16x8, b), c, 0, 0, 0);
}
template <int N> DI void wait_vm() { asm volatile("s_waitcnt vmcnt(%0)" ::"n"(N) : "memory"); }
DI void wait_lgkm0() { asm volatile("s_waitcnt lgkmcnt(0)" ::: "memory"); }
DI void bar() {
  asm volatile("" ::: "memory");
  __builtin_amdgcn_s_barrier();
  asm volatile("" ::: "memory");
}

template <int OFF> DI u32x2 tr_read(unsigned addr) {
  u32x2 r;
  asm volatile("ds_read_b64_tr_b16 %0, %1 offset:%2" : "=v"(r) : "v"(addr), "n"(OFF));
  return r;
}
template <int OFF> DI u32x4 lds_read128(unsigned addr) {
  u32x4 r;
  asm volatile("ds_read_b128 %0, %1 offset:%2" : "=v"(r) : "v"(addr), "n"(OFF));
  return r;
}
template <int N> DI void wait_lgkm() { asm volatile("s_waitcnt lgkmcnt(%0)" ::"n"(N) : "memory"); }
template <int I, int N, class F> DI void static_for(F&& f) {
  if constexpr (I < N) {
    f(std::integral_constant<int, I>{});
    static_for<I + 1, N>(f);
  }
}
DI void glds16(const void* g, void* l) {
  __builtin_amdgcn_global_load_lds((const unsigned*)g, (unsigned*)l, 16, 0, 0);
}

template <int KT, int NS> constexpr int iss_before(int kt) {
  int is = 2;
  for (int k = 0; k < kt; ++k) {
    int n = is + 2;
    if (n > k + NS) n = k + NS;
    if (n > KT) n = KT;
    is = n;
  }
  return is;
}
enum { M_QKV = 0, M_WO = 1, M_WIN = 2, M_SP = 3, M_OUT = 4 };

template <int BM, int BN, int WM, int WN, int WK, int SUB, int KT, int NS, int MODE, int LDA, int LDB, int PM, int PN>
__global__ __launch_bounds__(768, 3) void gemm_k(const u16* __restrict__ A, const u16* __restrict__ B,
                                                 const u16* __restrict__ e0, const u16* __restrict__ e1,
                                                 const float* __restrict__ partin, u16* __restrict__ o0,
                                                 u16* __restrict__ o1, u16* __restrict__ o2,
                                                 float* __restrict__ partout) {
  static_assert(WM * WN * WK == 8, "8 consumer waves");
  constexpr int AB = BM * 128, BB = BN * 128, SUBB = AB + BB, STG = SUB * SUBB;
  constexpr int AI = AB / 1024, BI = BB / 1024;
  static_assert(AI % 4 == 0, "A instr");
  constexpr int LA = AI / 4;
  constexpr int LBF = BI / 4, LBR = BI % 4;
  constexpr int MB = BM / WM / 16, NB = BN / WN / 16;
  constexpr int RING = NS * STG;
  constexpr int EXTRA = (MODE == M_SP) ? 16384 : 0;
  constexpr bool LNA = (MODE == M_QKV || MODE == M_WIN);
  __shared__ __attribute__((aligned(16))) char smem[RING + EXTRA];

  const int tid = threadIdx.x, lane = tid & 63;
  const int wid = __builtin_amdgcn_readfirstlane(tid >> 6);

  if constexpr (MODE == M_QKV) {
    if (blockIdx.x >= 192) {
      const int pb = blockIdx.x - 192;
      u32x4 accx = {0u, 0u, 0u, 0u};
      auto pf = [&](const u16* p, int n16) {
        const u32x4* q = (const u32x4*)p;
        for (int i = pb * 768 + tid; i < n16; i += 64 * 768 * 4) {
          u32x4 a = q[i];
          u32x4 b = (i + 64 * 768 < n16) ? q[i + 64 * 768] : a;
          u32x4 c = (i + 2 * 64 * 768 < n16) ? q[i + 2 * 64 * 768] : a;
          u32x4 d = (i + 3 * 64 * 768 < n16) ? q[i + 3 * 64 * 768] : a;
          accx ^= a ^ b ^ c ^ d;
        }
      };
      pf(e0, 512 * 512 / 8);
      pf(e1, 512 * 1792 / 8);
      pf((const u16*)partin, 2048 * 2048 / 8);
      pf((const u16*)partout, 896 * 512 / 8);
      asm volatile("" ::"v"(accx));
      return;
    }
  }
  constexpr int TN = (MODE == M_QKV ? 1536 : MODE == M_WIN ? 1792 : MODE == M_SP ? 896 : 512) / BN;
  constexpr int PXN = TN / PN;
  const int bid = blockIdx.x, xcd = bid & 7, jj = bid >> 3;
  const int tm = (xcd / PXN) * PM + jj / PN, tn = (xcd % PXN) * PN + jj % PN;
  const int m0 = tm * BM, n0 = tn * BN;

  if (wid >= 8) {
    const int lw = wid - 8;
    unsigned aoff[LA], boff[LBF + 1];
#pragma unroll
    for (int i = 0; i < LA; ++i) {
      int q = lw + 4 * i;
      int row = q * 8 + (lane >> 3), pos = lane & 7;
      int ch = pos ^ (row & 7);
      if constexpr (MODE == M_WO) aoff[i] = (unsigned)(((ch >> 2) * S_ + m0 + row) * 32 + (ch & 3) * 8);
      else aoff[i] = (unsigned)((m0 + row) * LDA + (ch << 3));
    }
#pragma unroll
    for (int i = 0; i < LBF; ++i) {
      int q = lw + 4 * i;
      int r = (q & 7) * 8 + (lane >> 3), pos = lane & 7;
      int h = ((r >> 1) & 1) | (((r >> 3) & 1) << 1);
      int ch = (((pos >> 1) ^ h) << 1) | (pos & 1);
      boff[i] = (unsigned)(r * LDB + n0 + (q >> 3) * 64 + ch * 8);
    }
    static_assert(LBR == 0, "BN % 32");
    const bool extraB = (lw < LBR);
    auto issue = [&](int kt, int slot) {
#pragma unroll
      for (int sb = 0; sb < SUB; ++sb) {
        const u16* a = A + (kt * SUB + sb) * (MODE == M_WO ? 2 * S_ * 32 : 64);
        const u16* b = B + (size_t)(kt * SUB + sb) * 64 * LDB;
        char* s = smem + slot * STG + sb * SUBB;
#pragma unroll
        for (int i = 0; i < LA; ++i) glds16(a + aoff[i], s + (lw + 4 * i) * 1024);
#pragma unroll
        for (int i = 0; i < LBF; ++i) glds16(b + boff[i], s + AB + (lw + 4 * i) * 1024);
        if (LBR != 0) {
          if (extraB) glds16(b + boff[LBF], s + AB + (lw + 4 * LBF) * 1024);
        }
      }
    };
    issue(0, 0);
    issue(1, 1);
    static_for<0, KT>([&](auto KTI) {
      constexpr int kt = KTI;
      constexpr int is0 = iss_before<KT, NS>(kt);
      constexpr int is1 = iss_before<KT, NS>(kt + 1);
      constexpr int later = is0 - 1 - kt;
      if (LBR != 0 && extraB) wait_vm<later * SUB * (LA + LBF + 1)>();
      else wait_vm<later * SUB * (LA + LBF)>();
      bar();
      if constexpr (is1 > is0) issue(is0, is0 % NS);
      if constexpr (is1 > is0 + 1) issue(is0 + 1, (is0 + 1) % NS);
    });
    return;
  }

  const int wk = wid % WK;
  const int wmn = wid / WK;
  const int wm = wmn / WN, wn = wmn % WN;
  const int r16 = lane & 15, g = lane >> 4;
  const int tokb = m0 + wm * (BM / WM);
  const int nbase = n0 + wn * (BN / WN);

  if constexpr (MODE == M_SP) {
    float* sr = (float*)(smem + RING);
    float* srm = sr + 2048;
#pragma unroll
    for (int j = 0; j < 2; ++j) {
      int row = 2 * tid + 1024 * j;
      f32x4 t = {0.f, 0.f, 0.f, 0.f};
#pragma unroll
      for (int p = 0; p < 7; ++p) {
        f32x4 v = *(const f32x4*)(partin + (size_t)(p * 2048 + row) * 2);
        t += v;
      }
      float mean0 = t[0] * (1.f / 896.f), mean1 = t[2] * (1.f / 896.f);
      float var0 = fmaxf(t[1] * (1.f / 896.f) - mean0 * mean0, 0.f);
      float var1 = fmaxf(t[3] * (1.f / 896.f) - mean1 * mean1, 0.f);
      float r0 = rsqrtf(var0 + 1e-5f), r1 = rsqrtf(var1 + 1e-5f);
      *(f32x2*)(sr + row) = (f32x2){r0, r1};
      *(f32x2*)(srm + row) = (f32x2){r0 * mean0, r1 * mean1};
    }
  }

  f32x4 acc[MB][NB];
#pragma unroll
  for (int i = 0; i < MB; ++i)
#pragma unroll
    for (int j = 0; j < NB; ++j) acc[i][j] = (f32x4){0.f, 0.f, 0.f, 0.f};
  float s1[MB], s2[MB], cs[NB];
  f32x2 cc2[MB];
#pragma unroll
  for (int i = 0; i < MB; ++i) { s1[i] = 0.f; s2[i] = 0.f; cc2[i] = (f32x2){0.f, 0.f}; }
#pragma unroll
  for (int j = 0; j < NB; ++j) cs[j] = 0.f;

  constexpr int NKS = 2 * SUB / WK;
  static_assert(NKS >= 1 && NKS <= 2, "NKS");
  static_assert(NKS == 1 || WK == 1, "NKS");
  const unsigned lbase = (unsigned)(size_t)smem;
  unsigned ard[NKS], bpo[4];
  {
    int q = (lane & 15) >> 2, p = lane & 3;
    int row = wm * (BM / WM) + r16;
#pragma unroll
    for (int kq = 0; kq < NKS; ++kq) {
      int kk = wk + WK * kq;
      int sub = kk >> 1, ks = kk & 1;
      ard[kq] = lbase + sub * SUBB + row * 128 + (((4 * ks + g) ^ (row & 7)) << 4);
    }
    int h = ((q >> 1) & 1) | ((g & 1) << 1);
    int kk0 = wk;
    unsigned bb = lbase + (kk0 >> 1) * SUBB + AB + ((kk0 & 1) * 32 + 8 * g + q) * 128 + p * 8;
    constexpr int NBW = BN / WN / 16;
    static_assert(NBW % 4 == 0 || NBW == 2, "wave n-blocks");
    int nb0 = wn * NBW;
#pragma unroll
    for (int pc = 0; pc < 4; ++pc) {
      int nb = nb0 + pc;
      bpo[pc] = bb + (nb >> 2) * 8192 + (((nb & 3) ^ h) << 5);
    }
  }
  unsigned strd = lbase + RING + ((wk & 1) * 32 + (wk >> 1) * 64 + g * 8) * 4;

  constexpr int ST = (MODE == M_SP) ? 4 : 0;
  constexpr int PER = ST + MB + 2 * NB;
  constexpr int RTOT = NKS * PER;
  constexpr int NB0 = (NB + 1) / 2;

  int slot = 0;
#pragma unroll 1
  for (int kt = 0; kt < KT; ++kt) {
    wait_lgkm0();
    bar();
    const unsigned so = slot * STG;
    u32x4 af[NKS][MB];
    u32x2 bl[NKS][NB], bh[NKS][NB];
    u32x4 st[4];
    static_for<0, NKS>([&](auto KQ) {
      if constexpr (MODE == M_SP) {
        st[0] = lds_read128<0>(strd);
        st[1] = lds_read128<16>(strd);
        st[2] = lds_read128<8192>(strd);
        st[3] = lds_read128<8208>(strd);
      }
      static_for<0, MB>([&](auto I) { af[KQ][I] = lds_read128<I * 2048>(ard[KQ] + so); });
      static_for<0, NB>([&](auto J) {
        constexpr int off = KQ * 4096 + (J >> 2) * 8192;
        bl[KQ][J] = tr_read<off>(bpo[J & 3] + so);
        bh[KQ][J] = tr_read<off + 512>(bpo[J & 3] + so);
      });
    });
    if constexpr (MODE == M_SP) strd += SUB * 256;
    const bool docs = LNA && ((kt % WM) == wm);
    static_for<0, NKS>([&](auto KQ) {
      constexpr int w0 = RTOT - (KQ * PER + ST + MB + 2 * NB0);
      constexpr int w1 = RTOT - (KQ + 1) * PER;
      constexpr int wa = RTOT - (KQ * PER + ST + MB);
      wait_lgkm<(wa > 15 ? 15 : wa)>();
      u32x4 a[MB];
#pragma unroll
      for (int i = 0; i < MB; ++i) {
        asm volatile("" : "+v"(af[KQ][i]));
        a[i] = af[KQ][i];
      }
      if constexpr (MODE == M_SP) {
        asm volatile("" : "+v"(st[0]), "+v"(st[1]), "+v"(st[2]), "+v"(st[3]));
        f32x4 r0 = __builtin_bit_cast(f32x4, st[0]), r1 = __builtin_bit_cast(f32x4, st[1]);
        f32x4 m0v = __builtin_bit_cast(f32x4, st[2]), m1v = __builtin_bit_cast(f32x4, st[3]);
#pragma unroll
        for (int i = 0; i < MB; ++i) {
          f32x2 w01 = {bflo(a[i][0]), bfhi(a[i][0])}, w23 = {bflo(a[i][1]), bfhi(a[i][1])};
          f32x2 w45 = {bflo(a[i][2]), bfhi(a[i][2])}, w67 = {bflo(a[i][3]), bfhi(a[i][3])};
          f32x2 c = cc2[i];
          c = __builtin_elementwise_fma(w01, (f32x2){m0v[0], m0v[1]}, c);
          c = __builtin_elementwise_fma(w23, (f32x2){m0v[2], m0v[3]}, c);
          c = __builtin_elementwise_fma(w45, (f32x2){m1v[0], m1v[1]}, c);
          c = __builtin_elementwise_fma(w67, (f32x2){m1v[2], m1v[3]}, c);
          cc2[i] = c;
          w01 *= (f32x2){r0[0], r0[1]}; w23 *= (f32x2){r0[2], r0[3]};
          w45 *= (f32x2){r1[0], r1[1]}; w67 *= (f32x2){r1[2], r1[3]};
          a[i][0] = pack2(w01[0], w01[1]);
          a[i][1] = pack2(w23[0], w23[1]);
          a[i][2] = pack2(w45[0], w45[1]);
          a[i][3] = pack2(w67[0], w67[1]);
        }
      }
      if constexpr (LNA) {
#pragma unroll
        for (int i = 0; i < MB; ++i) {
          if constexpr (WN == 2) {
#pragma unroll
            for (int e = 0; e < 4; ++e) s1[i] = dot2(a[i][e], wn ? a[i][e] : 0x3F803F80u, s1[i]);
          } else {
#pragma unroll
            for (int e = 0; e < 4; ++e) {
              s1[i] = dot2(a[i][e], 0x3F803F80u, s1[i]);
              s2[i] = dot2(a[i][e], a[i][e], s2[i]);
            }
          }
        }
      }
#pragma unroll
      for (int i = 0; i < MB; ++i) asm volatile("" : "+v"(a[i]));
      wait_lgkm<(w0 > 15 ? 15 : w0)>();
#pragma unroll
      for (int j = 0; j < NB0; ++j) {
        asm volatile("" : "+v"(bl[KQ][j]), "+v"(bh[KQ][j]));
        u32x4 b = {bl[KQ][j][0], bl[KQ][j][1], bh[KQ][j][0], bh[KQ][j][1]};
#pragma unroll
        for (int i = 0; i < MB; ++i) acc[i][j] = mfma16(b, a[i], acc[i][j]);
      }
      wait_lgkm<(w1 > 15 ? 15 : w1)>();
#pragma unroll
      for (int j = NB0; j < NB; ++j) {
        asm volatile("" : "+v"(bl[KQ][j]), "+v"(bh[KQ][j]));
        u32x4 b = {bl[KQ][j][0], bl[KQ][j][1], bh[KQ][j][0], bh[KQ][j][1]};
#pragma unroll
        for (int i = 0; i < MB; ++i) acc[i][j] = mfma16(b, a[i], acc[i][j]);
      }
      if constexpr (LNA) {
        if (docs) {
#pragma unroll
          for (int j = 0; j < NB; ++j) {
            cs[j] = dot2(bl[KQ][j][0], 0x3F803F80u, cs[j]);
            cs[j] = dot2(bl[KQ][j][1], 0x3F803F80u, cs[j]);
            cs[j] = dot2(bh[KQ][j][0], 0x3F803F80u, cs[j]);
            cs[j] = dot2(bh[KQ][j][1], 0x3F803F80u, cs[j]);
          }
        }
      }
    });
    ++slot; if (slot == NS) slot = 0;
  }

  if constexpr (MODE != M_QKV) {
    constexpr int RSF = BN * 4 + 16;
    constexpr int PLB = BM * RSF;
    constexpr int O_ROWP = WK * PLB;
    constexpr int O_COLP = O_ROWP + WK * BM * 8;
    constexpr int O_ROWF = O_COLP + 8 * BN * 4;
    constexpr int O_COLF = O_ROWF + BM * 8;
    static_assert(O_COLF + BN * 4 <= RING, "epilogue LDS");
    constexpr int CH = BN / 8;
    constexpr int LPR = (CH <= 8) ? 8 : 16;
    constexpr int RPP = 512 / LPR;
    constexpr int NPASS = BM / RPP;
    const int ec = tid & (LPR - 1), er = tid / LPR;
    const bool eact = ec < CH;
    u32x4 eop[NPASS];
    if constexpr (MODE != M_WIN) {
      constexpr int LDE = (MODE == M_SP) ? F_ : D_;
#pragma unroll
      for (int ps = 0; ps < NPASS; ++ps)
        if (eact) eop[ps] = *(const u32x4*)(e0 + (size_t)(m0 + er + ps * RPP) * LDE + n0 + ec * 8);
    }
    wait_lgkm0();
    bar();
    {
      char* pl = smem + wk * PLB + (wm * (BM / WM) + r16) * RSF + (wn * (BN / WN) + 4 * g) * 4;
#pragma unroll
      for (int i = 0; i < MB; ++i)
#pragma unroll
        for (int j = 0; j < NB; ++j) *(f32x4*)(pl + i * 16 * RSF + j * 64) = acc[i][j];
    }
    if constexpr (MODE == M_WIN || MODE == M_SP) {
#pragma unroll
      for (int i = 0; i < MB; ++i) {
        if constexpr (MODE == M_WIN && WN == 2) {
          float a = red4(s1[i]);
          if (g == 0) *(float*)(smem + O_ROWP + (wk * BM + wm * (BM / WM) + i * 16 + r16) * 8 + wn * 4) = a;
        } else {
          float a = (MODE == M_WIN) ? red4(s1[i]) : red4(cc2[i][0] + cc2[i][1]);
          float b = (MODE == M_WIN) ? red4(s2[i]) : 0.f;
          if (g == 0) *(f32x2*)(smem + O_ROWP + (wk * BM + wm * (BM / WM) + i * 16 + r16) * 8) = (f32x2){a, b};
        }
      }
    }
    if constexpr (MODE == M_WIN) {
#pragma unroll
      for (int j = 0; j < NB; ++j) {
        float v = red4(cs[j]);
        if (g == 0) *(float*)(smem + O_COLP + ((wm * WK + wk) * BN + wn * (BN / WN) + j * 16 + r16) * 4) = v;
      }
    }
    wait_lgkm0();
    bar();
    if constexpr (MODE == M_WIN) {
      if (tid < BN) {
        float c = 0.f;
#pragma unroll
        for (int w = 0; w < WM * WK; ++w) c += *(const float*)(smem + O_COLP + (w * BN + tid) * 4);
        *(float*)(smem + O_COLF + tid * 4) = c;
      } else if (tid >= 128 && tid < 128 + BM) {
        int row = tid - 128;
        f32x2 t = {0.f, 0.f};
#pragma unroll
        for (int k = 0; k < WK; ++k) t += *(const f32x2*)(smem + O_ROWP + (k * BM + row) * 8);
        float mean = t[0] * (1.f / 512.f);
        float var = fmaxf(t[1] * (1.f / 512.f) - mean * mean, 0.f);
        *(f32x2*)(smem + O_ROWF + row * 8) = (f32x2){mean, rsqrtf(var + 1e-5f)};
      }
      wait_lgkm0();
      bar();
    }
    f32x4 cf0 = {0.f, 0.f, 0.f, 0.f}, cf1 = cf0;
    if constexpr (MODE == M_WIN) {
      if (eact) {
        cf0 = *(const f32x4*)(smem + O_COLF + ec * 32);
        cf1 = *(const f32x4*)(smem + O_COLF + ec * 32 + 16);
      }
    }
#pragma unroll
    for (int ps = 0; ps < NPASS; ++ps) {
      const int row = er + ps * RPP;
      const int tok = m0 + row;
      float st1 = 0.f, st2 = 0.f;
      if (eact) {
        f32x4 v0 = *(const f32x4*)(smem + row * RSF + ec * 32);
        f32x4 v1 = *(const f32x4*)(smem + row * RSF + ec * 32 + 16);
#pragma unroll
        for (int k = 1; k < WK; ++k) {
          v0 += *(const f32x4*)(smem + k * PLB + row * RSF + ec * 32);
          v1 += *(const f32x4*)(smem + k * PLB + row * RSF + ec * 32 + 16);
        }
        float v[8] = {v0[0], v0[1], v0[2], v0[3], v1[0], v1[1], v1[2], v1[3]};
        u32x4 ov;
        if constexpr (MODE == M_WO || MODE == M_OUT) {
#pragma unroll
          for (int e = 0; e < 4; ++e)
            ov[e] = pack2(bflo(eop[ps][e]) + rbf(v[2 * e]), bfhi(eop[ps][e]) + rbf(v[2 * e + 1]));
          *(u32x4*)(o0 + (size_t)tok * D_ + n0 + ec * 8) = ov;
        } else if constexpr (MODE == M_SP) {
          float c = 0.f;
#pragma unroll
          for (int k = 0; k < WK; ++k) c += *(const float*)(smem + O_ROWP + (k * BM + row) * 8);
          const float bias = bflo((unsigned)e1[tok]);
#pragma unroll
          for (int e = 0; e < 4; ++e) {
            float g0 = rbf(rbf(v[2 * e] - c) + bias), g1 = rbf(rbf(v[2 * e + 1] - c) + bias);
            ov[e] = pack2(bflo(eop[ps][e]) * g0, bfhi(eop[ps][e]) * g1);
          }
          *(u32x4*)(o0 + (size_t)tok * FH_ + n0 + ec * 8) = ov;
        } else {
          f32x2 ms = *(const f32x2*)(smem + O_ROWF + row * 8);
          float cf[8] = {cf0[0], cf0[1], cf0[2], cf0[3], cf1[0], cf1[1], cf1[2], cf1[3]};
          float zz[8];
#pragma unroll
          for (int e = 0; e < 8; ++e) {
            float x = rbf(ms[1] * (v[e] - ms[0] * cf[e]));
            float a = x * fmaf(x * x, -0.10294324f, -2.3022082f);
            float t = __builtin_amdgcn_exp2f(a);
            zz[e] = x * __builtin_amdgcn_rcpf(1.f + t);
          }
#pragma unroll
          for (int e = 0; e < 4; ++e) ov[e] = pack2(zz[2 * e], zz[2 * e + 1]);
          *(u32x4*)(o0 + (size_t)tok * F_ + n0 + ec * 8) = ov;
          if (n0 >= FH_) {
#pragma unroll
            for (int e = 0; e < 4; ++e) {
              st1 = dot2(ov[e], 0x3F803F80u, st1);
              st2 = dot2(ov[e], ov[e], st2);
            }
          }
        }
      }
      if constexpr (MODE == M_WIN) {
        if (n0 >= FH_) {
          static_assert(LPR == 16, "row reduce");
          st1 = rowsum16(st1);
          st2 = rowsum16(st2);
          if (ec == 15) *(f32x2*)(partout + (size_t)(((n0 - FH_) / BN) * 2048 + tok) * 2) = (f32x2){st1, st2};
        }
      }
    }
    return;
  }
  if constexpr (MODE == M_QKV) {
    static_assert(WK == 1 && WN == 2 && MB == 2, "qkv epilogue");
    wait_lgkm0();
    bar();
    float* csl = (float*)smem;
    f32x4* ex = (f32x4*)(smem + 4096);
#pragma unroll
    for (int j = 0; j < NB; ++j) {
      float v = red4(cs[j]);
      if (g == 0) csl[wid * (NB * 16) + j * 16 + r16] = v;
    }
    ex[wid * 64 + lane] = (f32x4){s1[0], 0.f, s1[1], 0.f};
    wait_lgkm0();
    bar();
    {
      f32x4 o = ex[(wid ^ 1) * 64 + lane];
      float m0_ = wn ? o[0] : s1[0], q0_ = wn ? s1[0] : o[0];
      float m1_ = wn ? o[2] : s1[1], q1_ = wn ? s1[1] : o[2];
      s1[0] = m0_; s2[0] = q0_;
      s1[1] = m1_; s2[1] = q1_;
    }
    float mean[MB], rstd[MB];
#pragma unroll
    for (int i = 0; i < MB; ++i) {
      float a = red4(s1[i]), b = red4(s2[i]);
      mean[i] = a * (1.f / 512.f);
      float var = fmaxf(b * (1.f / 512.f) - mean[i] * mean[i], 0.f);
      rstd[i] = rsqrtf(var + 1e-5f);
    }
    const int which = n0 >> 9;
#pragma unroll
    for (int j = 0; j < NB; ++j) {
      f32x4 c4 = {0.f, 0.f, 0.f, 0.f};
#pragma unroll
      for (int w = 0; w < 4; ++w) c4 += *(const f32x4*)(csl + (w * 2 + wn) * (NB * 16) + j * 16 + 4 * g);
      const int n = nbase + j * 16;
      const int head = (n & 511) >> 4;
      if (which < 2) {
        u16* dst = (which == 0 ? o0 : o1);
        const float sc = (which == 0) ? 0.36067376022224085f : 1.f;
#pragma unroll
        for (int i = 0; i < MB; ++i) {
          int tok = tokb + i * 16 + r16;
          float rs = rstd[i] * sc;
          float v0 = rs * (acc[i][j][0] - mean[i] * c4[0]);
          float v1 = rs * (acc[i][j][1] - mean[i] * c4[1]);
          float v2 = rs * (acc[i][j][2] - mean[i] * c4[2]);
          float v3 = rs * (acc[i][j][3] - mean[i] * c4[3]);
          *(u32x2*)(dst + ((size_t)head * S_ + tok) * 16 + 4 * g) = (u32x2){pack2(v0, v1), pack2(v2, v3)};
        }
      } else {
        u16* vt = (u16*)(smem + 16384 + (wid * NB + j) * (MB * 512));
#pragma unroll
        for (int i = 0; i < MB; ++i) {
#pragma unroll
          for (int e = 0; e < 4; ++e) {
            float v = rstd[i] * (acc[i][j][e] - mean[i] * c4[e]);
            __bf16 bv = (__bf16)v;
            vt[(4 * g + e) * (MB * 16) + i * 16 + r16] = __builtin_bit_cast(u16, bv);
          }
        }
        int d = lane >> 2, c = lane & 3;
        u32x4 v = *(const u32x4*)(vt + d * 32 + c * 8);
        *(u32x4*)(o2 + ((size_t)head * 16 + d) * S_ + tokb + c * 8) = v;
      }
    }
  }
}

constexpr int AT_KB = 384 * 32;
constexpr int AT_VS = 784;
constexpr int AT_VB = 32 * AT_VS;
constexpr int AT_HB = AT_KB + AT_VB;
__global__ __launch_bounds__(512, 2) void attn_k(const u16* __restrict__ Q, const u16* __restrict__ K,
                                                 const u16* __restrict__ Vt, u16* __restrict__ O2) {
  __shared__ __attribute__((aligned(16))) char smem[2 * AT_HB];
  const int tid = threadIdx.x, lane = tid & 63;
  const int wid = __builtin_amdgcn_readfirstlane(tid >> 6);
  const int hp = blockIdx.x >> 4, qb = blockIdx.x & 15;
  const int hl = wid >> 2;
  const int head = hp * 2 + hl;
  const int qt = qb * 4 + (wid & 3);
  const int q0 = qt * 32;
  const int kw0 = qb * 128 - 256;
  const int i = lane & 31, hh = lane >> 5;

  {
    const int t = tid & 255;
    const u16* Kh = K + (size_t)head * S_ * 16;
    const u16* Vh = Vt + (size_t)head * 16 * S_;
    char* kl = smem + hl * AT_HB;
    char* vl = kl + AT_KB;
    u32x4 kv[3], vv[3];
#pragma unroll
    for (int r = 0; r < 3; ++r) {
      int c = t + 256 * r;
      int key = kw0 + (c >> 1); key = key < 0 ? 0 : key;
      kv[r] = *(const u32x4*)(Kh + key * 16 + (c & 1) * 8);
      int d = c / 48, cc = c % 48;
      int k2 = kw0 + cc * 8; k2 = k2 < 0 ? 0 : k2;
      vv[r] = *(const u32x4*)(Vh + d * S_ + k2);
    }
#pragma unroll
    for (int r = 0; r < 3; ++r) {
      int c = t + 256 * r;
      int d = c / 48, cc = c % 48;
      unsigned f = (d == 0) ? 0x3F803F80u : 0u;
      *(u32x4*)(vl + (16 + d) * AT_VS + cc * 16) = (u32x4){f, f, f, f};
    }
#pragma unroll
    for (int r = 0; r < 3; ++r) {
      int c = t + 256 * r;
      int row = c >> 1;
      *(u32x4*)(kl + row * 32 + (((c & 1) ^ ((row >> 3) & 1)) << 4)) = kv[r];
      int d = c / 48, cc = c % 48;
      *(u32x4*)(vl + d * AT_VS + cc * 16) = vv[r];
    }
  }
  u32x4 qf = *(const u32x4*)(Q + ((size_t)head * S_ + q0 + i) * 16 + hh * 8);

  const int t0 = (q0 >= 256) ? 0 : (8 - qt);
  const int pi = ((i >> 2) & 1) * 16 + ((i >> 4) & 1) * 8 + ((i >> 3) & 1) * 4 + (i & 3);
  const float NEG = -1e30f;
  f32x16 c0, c8, zero;
#pragma unroll
  for (int r = 0; r < 16; ++r) {
    int kap = 16 * hh + r;
    zero[r] = 0.f;
    c0[r] = (kap > i) ? 0.f : NEG;
    c8[r] = (kap <= i) ? 0.f : NEG;
  }
  __syncthreads();

  const int wrow = (wid & 3) * 32;
  const char* kr = smem + hl * AT_HB + (wrow + pi) * 32 + ((hh ^ ((pi >> 3) & 1)) << 4);
  const char* vr = smem + hl * AT_HB + AT_KB + i * AT_VS + (wrow + 16 * hh) * 2;

  u32x4 kf[9];
#pragma unroll
  for (int t = 0; t < 9; ++t) kf[t] = *(const u32x4*)(kr + t * 1024);

  float mx = NEG;
#pragma unroll
  for (int t = 0; t < 9; ++t) {
    if (t >= t0) {
      f32x16 s = mfma32(kf[t], qf, t == 0 ? c0 : (t == 8 ? c8 : zero));
#pragma unroll
      for (int r = 0; r < 16; r += 2) mx = fmaxf(fmaxf(mx, s[r]), s[r + 1]);
    }
  }
  {
    unsigned u = __float_as_uint(mx);
    auto r = __builtin_amdgcn_permlane32_swap(u, u, false, false);
    mx = fmaxf(__uint_as_float(r[0]), __uint_as_float(r[1]));
  }
  f32x16 cm;
#pragma unroll
  for (int r = 0; r < 16; ++r) { cm[r] = -mx; c0[r] -= mx; c8[r] -= mx; }

  f32x16 oacc = zero;
#pragma unroll
  for (int t = 0; t < 9; ++t) {
    if (t >= t0) {
      u32x4 v0 = *(const u32x4*)(vr + t * 64);
      u32x4 v1 = *(const u32x4*)(vr + t * 64 + 16);
      f32x16 s = mfma32(kf[t], qf, t == 0 ? c0 : (t == 8 ? c8 : cm));
      float p[16];
#pragma unroll
      for (int r = 0; r < 16; ++r) p[r] = __builtin_amdgcn_exp2f(s[r]);
      u32x4 p0 = {pack2(p[0], p[1]), pack2(p[2], p[3]), pack2(p[4], p[5]), pack2(p[6], p[7])};
      u32x4 p1 = {pack2(p[8], p[9]), pack2(p[10], p[11]), pack2(p[12], p[13]), pack2(p[14], p[15])};
      oacc = mfma32(v0, p0, oacc);
      oacc = mfma32(v1, p1, oacc);
    }
  }
  float l;
  {
    unsigned u = __float_as_uint(oacc[8]);
    auto r = __builtin_amdgcn_permlane32_swap(u, u, false, false);
    l = __uint_as_float(r[0]) + __uint_as_float(r[1]);
  }
  float inv = 1.f / l;
  __syncthreads();
  {
    char* ot = smem + ((wid & 3) * 32 + i) * 64 + hl * 32 + hh * 8;
    *(u32x2*)(ot) = (u32x2){pack2(oacc[0] * inv, oacc[1] * inv), pack2(oacc[2] * inv, oacc[3] * inv)};
    *(u32x2*)(ot + 16) = (u32x2){pack2(oacc[4] * inv, oacc[5] * inv), pack2(oacc[6] * inv, oacc[7] * inv)};
  }
  __syncthreads();
  *(u32x4*)(O2 + ((size_t)hp * S_ + qb * 128) * 32 + tid * 8) = *(const u32x4*)(smem + tid * 16);
}

extern "C" void kernel_launch(void* const* d_in, const int* in_sizes, int n_in, void* d_out, int out_size,
                              void* d_ws, size_t ws_size, hipStream_t stream) {
  const u16* x = (const u16*)d_in[0];
  const u16* wqkv = (const u16*)d_in[1];
  const u16* wo = (const u16*)d_in[2];
  const u16* w_in = (const u16*)d_in[4];
  const u16* w_sp = (const u16*)d_in[5];
  const u16* b_sp = (const u16*)d_in[6];
  const u16* w_out = (const u16*)d_in[7];
  u16* out = (u16*)d_out;

  char* ws = (char*)d_ws;
  u16* Qb = (u16*)(ws);
  u16* Kb = (u16*)(ws + (2u << 20));
  u16* Vb = (u16*)(ws + (4u << 20));
  u16* Ob = (u16*)(ws + (6u << 20));
  u16* Hb = (u16*)(ws + (8u << 20));
  u16* Zb = (u16*)(ws + (10u << 20));
  u16* Ub = (u16*)(ws + (18u << 20));
  float* Pb = (float*)(ws + (22u << 20));

  gemm_k<128, 128, 4, 2, 1, 1, 8, 4, M_QKV, 512, 1536, 4, 6><<<256, 768, 0, stream>>>(
      x, wqkv, wo, w_in, (const float*)w_sp, Qb, Kb, Vb, (float*)w_out);
  attn_k<<<256, 512, 0, stream>>>(Qb, Kb, Vb, Ob);
  gemm_k<64, 64, 4, 2, 1, 1, 8, 8, M_WO, 512, 512, 4, 8><<<256, 768, 0, stream>>>(
      Ob, wo, x, nullptr, nullptr, Hb, nullptr, nullptr, nullptr);
  gemm_k<128, 128, 4, 2, 1, 1, 8, 4, M_WIN, 512, 1792, 4, 7><<<224, 768, 0, stream>>>(
      Hb, w_in, nullptr, nullptr, nullptr, Zb, nullptr, nullptr, Pb);
  gemm_k<64, 128, 2, 1, 4, 2, 16, 3, M_SP, 2048, 1792, 4, 7><<<224, 768, 0, stream>>>(
      w_sp, Zb + FH_, Zb, b_sp, Pb, Ub, nullptr, nullptr, nullptr);
  gemm_k<64, 64, 4, 2, 1, 1, 14, 8, M_OUT, 896, 512, 4, 8><<<256, 768, 0, stream>>>(
      Ub, w_out, Hb, nullptr, nullptr, out, nullptr, nullptr, nullptr);
}
```

```cpp
#include <hip/hip_runtime.h>
#include <hip/hip_bf16.h>
#include <type_traits>

typedef unsigned short u16;
typedef short s16x8 __attribute__((ext_vector_type(8)));
typedef short s16x4 __attribute__((ext_vector_type(4)));
typedef float f32x4 __attribute__((ext_vector_type(4)));
typedef float f32x2 __attribute__((ext_vector_type(2)));
typedef float f32x16 __attribute__((ext_vector_type(16)));
typedef __bf16 bf16x2 __attribute__((ext_vector_type(2)));
typedef __bf16 bf16x8 __attribute__((ext_vector_type(8)));
typedef unsigned u32x4 __attribute__((ext_vector_type(4)));
typedef unsigned u32x2 __attribute__((ext_vector_type(2)));

#define DI __device__ __forceinline__
#define SCHED_FENCE __builtin_amdgcn_sched_barrier(0)

constexpr int S_ = 2048, D_ = 512, H_ = 32, DH_ = 16, F_ = 1792, FH_ = 896;

DI unsigned pack2(float a, float b) {
  bf16x2 v = {(__bf16)a, (__bf16)b};
  return __builtin_bit_cast(unsigned, v);
}
DI float bflo(unsigned u) { return __uint_as_float(u << 16); }
DI float bfhi(unsigned u) { return __uint_as_float(u & 0xffff0000u); }
DI float rbf(float a) {
  __bf16 v = (__bf16)a;
  return __uint_as_float(((unsigned)__builtin_bit_cast(u16, v)) << 16);
}
DI float red4(float v) {
  unsigned u = __float_as_uint(v);
  auto r = __builtin_amdgcn_permlane16_swap(u, u, false, false);
  float a = __uint_as_float(r[0]) + __uint_as_float(r[1]);
  unsigned w = __float_as_uint(a);
  auto q = __builtin_amdgcn_permlane32_swap(w, w, false, false);
  return __uint_as_float(q[0]) + __uint_as_float(q[1]);
}
DI float rowsum16(float v) {
  v += __int_as_float(__builtin_amdgcn_update_dpp(0, __float_as_int(v), 0x111, 0xf, 0xf, true));
  v += __int_as_float(__builtin_amdgcn_update_dpp(0, __float_as_int(v), 0x112, 0xf, 0xf, true));
  v += __int_as_float(__builtin_amdgcn_update_dpp(0, __float_as_int(v), 0x114, 0xf, 0xf, true));
  v += __int_as_float(__builtin_amdgcn_update_dpp(0, __float_as_int(v), 0x118, 0xf, 0xf, true));
  return v;
}
DI float dot2(unsigned a, unsigned b, float c) {
  return __builtin_amdgcn_fdot2_f32_bf16(__builtin_bit_cast(bf16x2, a), __builtin_bit_cast(bf16x2, b), c, false);
}
DI f32x4 mfma16(u32x4 a, u32x4 b, f32x4 c) {
  return __builtin_amdgcn_mfma_f32_16x16x32_bf16(__builtin_bit_cast(bf16x8, a), __builtin_bit_cast(bf16x8, b), c, 0, 0, 0);
}
DI f32x16 mfma32(u32x4 a, u32x4 b, f32x16 c) {
  return __builtin_amdgcn_mfma_f32_32x32x16_bf16(__builtin_bit_cast(bf16x8, a), __builtin_bit_cast(bf16x8, b), c, 0, 0, 0);
}
template <int N> DI void wait_vm() { asm volatile("s_waitcnt vmcnt(%0)" ::"n"(N) : "memory"); }
DI void wait_lgkm0() { asm volatile("s_waitcnt lgkmcnt(0)" ::: "memory"); }
DI void bar() {
  asm volatile("" ::: "memory");
  __builtin_amdgcn_s_barrier();
  asm volatile("" ::: "memory");
}

template <int OFF> DI u32x2 tr_read(unsigned addr) {
  u32x2 r;
  asm volatile("ds_read_b64_tr_b16 %0, %1 offset:%2" : "=v"(r) : "v"(addr), "n"(OFF));
  return r;
}
template <int OFF> DI u32x4 lds_read128(unsigned addr) {
  u32x4 r;
  asm volatile("ds_read_b128 %0, %1 offset:%2" : "=v"(r) : "v"(addr), "n"(OFF));
  return r;
}
template <int N> DI void wait_lgkm() { asm volatile("s_waitcnt lgkmcnt(%0)" ::"n"(N) : "memory"); }
template <int I, int N, class F> DI void static_for(F&& f) {
  if constexpr (I < N) {
    f(std::integral_constant<int, I>{});
    static_for<I + 1, N>(f);
  }
}
DI void glds16(const void* g, void* l) {
  __builtin_amdgcn_global_load_lds((const unsigned*)g, (unsigned*)l, 16, 0, 0);
}

template <int KT, int NS> constexpr int iss_before(int kt) {
  int is = 2;
  for (int k = 0; k < kt; ++k) {
    int n = is + 2;
    if (n > k + NS) n = k + NS;
    if (n > KT) n = KT;
    is = n;
  }
  return is;
}
enum { M_QKV = 0, M_WO = 1, M_WIN = 2, M_SP = 3, M_OUT = 4 };

template <int BM, int BN, int WM, int WN, int WK, int SUB, int KT, int NS, int MODE, int LDA, int LDB, int PM, int PN>
__global__ __launch_bounds__(768, 3) void gemm_k(const u16* __restrict__ A, const u16* __restrict__ B,
                                                 const u16* __restrict__ e0, const u16* __restrict__ e1,
                                                 const float* __restrict__ partin, u16* __restrict__ o0,
                                                 u16* __restrict__ o1, u16* __restrict__ o2,
                                                 float* __restrict__ partout) {
  static_assert(WM * WN * WK == 8, "8 consumer waves");
  constexpr int AB = BM * 128, BB = BN * 128, SUBB = AB + BB, STG = SUB * SUBB;
  constexpr int AI = AB / 1024, BI = BB / 1024;
  static_assert(AI % 4 == 0, "A instr");
  constexpr int LA = AI / 4;
  constexpr int LBF = BI / 4, LBR = BI % 4;
  constexpr int MB = BM / WM / 16, NB = BN / WN / 16;
  constexpr int RING = NS * STG;
  constexpr int EXTRA = (MODE == M_SP) ? 16384 : 0;
  constexpr bool LNA = (MODE == M_QKV || MODE == M_WIN);
  __shared__ __attribute__((aligned(16))) char smem[RING + EXTRA];

  const int tid = threadIdx.x, lane = tid & 63;
  const int wid = __builtin_amdgcn_readfirstlane(tid >> 6);

  if constexpr (MODE == M_QKV) {
    if (blockIdx.x >= 192) {
      const int pb = blockIdx.x - 192;
      u32x4 accx = {0u, 0u, 0u, 0u};
      auto pf = [&](const u16* p, int n16) {
        const u32x4* q = (const u32x4*)p;
        for (int i = pb * 768 + tid; i < n16; i += 64 * 768 * 4) {
          u32x4 a = q[i];
          u32x4 b = (i + 64 * 768 < n16) ? q[i + 64 * 768] : a;
          u32x4 c = (i + 2 * 64 * 768 < n16) ? q[i + 2 * 64 * 768] : a;
          u32x4 d = (i + 3 * 64 * 768 < n16) ? q[i + 3 * 64 * 768] : a;
          accx ^= a ^ b ^ c ^ d;
        }
      };
      pf(e0, 512 * 512 / 8);
      pf(e1, 512 * 1792 / 8);
      asm volatile("" ::"v"(accx));
      return;
    }
  }
  constexpr int TN = (MODE == M_QKV ? 1536 : MODE == M_WIN ? 1792 : MODE == M_SP ? 896 : 512) / BN;
  constexpr int PXN = TN / PN;
  const int bid = blockIdx.x, xcd = bid & 7, jj = bid >> 3;
  const int tm = (xcd / PXN) * PM + jj / PN, tn = (xcd % PXN) * PN + jj % PN;
  const int m0 = tm * BM, n0 = tn * BN;

  if (wid >= 8) {
    const int lw = wid - 8;
    unsigned aoff[LA], boff[LBF + 1];
#pragma unroll
    for (int i = 0; i < LA; ++i) {
      int q = lw + 4 * i;
      int row = q * 8 + (lane >> 3), pos = lane & 7;
      int ch = pos ^ (row & 7);
      if constexpr (MODE == M_WO) aoff[i] = (unsigned)(((ch >> 2) * S_ + m0 + row) * 32 + (ch & 3) * 8);
      else aoff[i] = (unsigned)((m0 + row) * LDA + (ch << 3));
    }
#pragma unroll
    for (int i = 0; i < LBF; ++i) {
      int q = lw + 4 * i;
      int r = (q & 7) * 8 + (lane >> 3), pos = lane & 7;
      int h = ((r >> 1) & 1) | (((r >> 3) & 1) << 1);
      int ch = (((pos >> 1) ^ h) << 1) | (pos & 1);
      boff[i] = (unsigned)(r * LDB + n0 + (q >> 3) * 64 + ch * 8);
    }
    static_assert(LBR == 0, "BN % 32");
    const bool extraB = (lw < LBR);
    auto issue = [&](int kt, int slot) {
#pragma unroll
      for (int sb = 0; sb < SUB; ++sb) {
        const u16* a = A + (kt * SUB + sb) * (MODE == M_WO ? 2 * S_ * 32 : 64);
        const u16* b = B + (size_t)(kt * SUB + sb) * 64 * LDB;
        char* s = smem + slot * STG + sb * SUBB;
#pragma unroll
        for (int i = 0; i < LA; ++i) glds16(a + aoff[i], s + (lw + 4 * i) * 1024);
#pragma unroll
        for (int i = 0; i < LBF; ++i) glds16(b + boff[i], s + AB + (lw + 4 * i) * 1024);
        if (LBR != 0) {
          if (extraB) glds16(b + boff[LBF], s + AB + (lw + 4 * LBF) * 1024);
        }
      }
    };
    issue(0, 0);
    issue(1, 1);
    static_for<0, KT>([&](auto KTI) {
      constexpr int kt = KTI;
      constexpr int is0 = iss_before<KT, NS>(kt);
      constexpr int is1 = iss_before<KT, NS>(kt + 1);
      constexpr int later = is0 - 1 - kt;
      if (LBR != 0 && extraB) wait_vm<later * SUB * (LA + LBF + 1)>();
      else wait_vm<later * SUB * (LA + LBF)>();
      bar();
      if constexpr (is1 > is0) issue(is0, is0 % NS);
      if constexpr (is1 > is0 + 1) issue(is0 + 1, (is0 + 1) % NS);
    });
    return;
  }

  const int wk = wid % WK;
  const int wmn = wid / WK;
  const int wm = wmn / WN, wn = wmn % WN;
  const int r16 = lane & 15, g = lane >> 4;
  const int tokb = m0 + wm * (BM / WM);
  const int nbase = n0 + wn * (BN / WN);

  if constexpr (MODE == M_SP) {
    float* sr = (float*)(smem + RING);
    float* srm = sr + 2048;
#pragma unroll
    for (int j = 0; j < 2; ++j) {
      int row = 2 * tid + 1024 * j;
      f32x4 t = {0.f, 0.f, 0.f, 0.f};
#pragma unroll
      for (int p = 0; p < 7; ++p) {
        f32x4 v = *(const f32x4*)(partin + (size_t)(p * 2048 + row) * 2);
        t += v;
      }
      float mean0 = t[0] * (1.f / 896.f), mean1 = t[2] * (1.f / 896.f);
      float var0 = fmaxf(t[1] * (1.f / 896.f) - mean0 * mean0, 0.f);
      float var1 = fmaxf(t[3] * (1.f / 896.f) - mean1 * mean1, 0.f);
      float r0 = rsqrtf(var0 + 1e-5f), r1 = rsqrtf(var1 + 1e-5f);
      *(f32x2*)(sr + row) = (f32x2){r0, r1};
      *(f32x2*)(srm + row) = (f32x2){r0 * mean0, r1 * mean1};
    }
  }

  f32x4 acc[MB][NB];
#pragma unroll
  for (int i = 0; i < MB; ++i)
#pragma unroll
    for (int j = 0; j < NB; ++j) acc[i][j] = (f32x4){0.f, 0.f, 0.f, 0.f};
  float s1[MB], s2[MB], cs[NB];
  f32x2 cc2[MB];
#pragma unroll
  for (int i = 0; i < MB; ++i) { s1[i] = 0.f; s2[i] = 0.f; cc2[i] = (f32x2){0.f, 0.f}; }
#pragma unroll
  for (int j = 0; j < NB; ++j) cs[j] = 0.f;

  constexpr int NKS = 2 * SUB / WK;
  static_assert(NKS >= 1 && NKS <= 2, "NKS");
  static_assert(NKS == 1 || WK == 1, "NKS");
  const unsigned lbase = (unsigned)(size_t)smem;
  unsigned ard[NKS], bpo[4];
  {
    int q = (lane & 15) >> 2, p = lane & 3;
    int row = wm * (BM / WM) + r16;
#pragma unroll
    for (int kq = 0; kq < NKS; ++kq) {
      int kk = wk + WK * kq;
      int sub = kk >> 1, ks = kk & 1;
      ard[kq] = lbase + sub * SUBB + row * 128 + (((4 * ks + g) ^ (row & 7)) << 4);
    }
    int h = ((q >> 1) & 1) | ((g & 1) << 1);
    int kk0 = wk;
    unsigned bb = lbase + (kk0 >> 1) * SUBB + AB + ((kk0 & 1) * 32 + 8 * g + q) * 128 + p * 8;
    constexpr int NBW = BN / WN / 16;
    static_assert(NBW % 4 == 0 || NBW == 2, "wave n-blocks");
    int nb0 = wn * NBW;
#pragma unroll
    for (int pc = 0; pc < 4; ++pc) {
      int nb = nb0 + pc;
      bpo[pc] = bb + (nb >> 2) * 8192 + (((nb & 3) ^ h) << 5);
    }
  }
  unsigned strd = lbase + RING + ((wk & 1) * 32 + (wk >> 1) * 64 + g * 8) * 4;

  constexpr int ST = (MODE == M_SP) ? 4 : 0;
  constexpr int PER = ST + MB + 2 * NB;
  constexpr int RTOT = NKS * PER;
  constexpr int NB0 = (NB + 1) / 2;

  int slot = 0;
#pragma unroll 1
  for (int kt = 0; kt < KT; ++kt) {
    wait_lgkm0();
    bar();
    const unsigned so = slot * STG;
    u32x4 af[NKS][MB];
    u32x2 bl[NKS][NB], bh[NKS][NB];
    u32x4 st[4];
    static_for<0, NKS>([&](auto KQ) {
      if constexpr (MODE == M_SP) {
        st[0] = lds_read128<0>(strd);
        st[1] = lds_read128<16>(strd);
        st[2] = lds_read128<8192>(strd);
        st[3] = lds_read128<8208>(strd);
      }
      static_for<0, MB>([&](auto I) { af[KQ][I] = lds_read128<I * 2048>(ard[KQ] + so); });
      static_for<0, NB>([&](auto J) {
        constexpr int off = KQ * 4096 + (J >> 2) * 8192;
        bl[KQ][J] = tr_read<off>(bpo[J & 3] + so);
        bh[KQ][J] = tr_read<off + 512>(bpo[J & 3] + so);
      });
    });
    if constexpr (MODE == M_SP) strd += SUB * 256;
    const bool docs = LNA && ((kt % WM) == wm);
    static_for<0, NKS>([&](auto KQ) {
      constexpr int w0 = RTOT - (KQ * PER + ST + MB + 2 * NB0);
      constexpr int w1 = RTOT - (KQ + 1) * PER;
      constexpr int wa = RTOT - (KQ * PER + ST + MB);
      wait_lgkm<(wa > 15 ? 15 : wa)>();
      u32x4 a[MB];
#pragma unroll
      for (int i = 0; i < MB; ++i) {
        asm volatile("" : "+v"(af[KQ][i]));
        a[i] = af[KQ][i];
      }
      if constexpr (MODE == M_SP) {
        asm volatile("" : "+v"(st[0]), "+v"(st[1]), "+v"(st[2]), "+v"(st[3]));
        f32x4 r0 = __builtin_bit_cast(f32x4, st[0]), r1 = __builtin_bit_cast(f32x4, st[1]);
        f32x4 m0v = __builtin_bit_cast(f32x4, st[2]), m1v = __builtin_bit_cast(f32x4, st[3]);
#pragma unroll
        for (int i = 0; i < MB; ++i) {
          f32x2 w01 = {bflo(a[i][0]), bfhi(a[i][0])}, w23 = {bflo(a[i][1]), bfhi(a[i][1])};
          f32x2 w45 = {bflo(a[i][2]), bfhi(a[i][2])}, w67 = {bflo(a[i][3]), bfhi(a[i][3])};
          f32x2 c = cc2[i];
          c = __builtin_elementwise_fma(w01, (f32x2){m0v[0], m0v[1]}, c);
          c = __builtin_elementwise_fma(w23, (f32x2){m0v[2], m0v[3]}, c);
          c = __builtin_elementwise_fma(w45, (f32x2){m1v[0], m1v[1]}, c);
          c = __builtin_elementwise_fma(w67, (f32x2){m1v[2], m1v[3]}, c);
          cc2[i] = c;
          w01 *= (f32x2){r0[0], r0[1]}; w23 *= (f32x2){r0[2], r0[3]};
          w45 *= (f32x2){r1[0], r1[1]}; w67 *= (f32x2){r1[2], r1[3]};
          a[i][0] = pack2(w01[0], w01[1]);
          a[i][1] = pack2(w23[0], w23[1]);
          a[i][2] = pack2(w45[0], w45[1]);
          a[i][3] = pack2(w67[0], w67[1]);
        }
      }
      if constexpr (LNA) {
#pragma unroll
        for (int i = 0; i < MB; ++i) {
          if constexpr (WN == 2) {
#pragma unroll
            for (int e = 0; e < 4; ++e) s1[i] = dot2(a[i][e], wn ? a[i][e] : 0x3F803F80u, s1[i]);
          } else {
#pragma unroll
            for (int e = 0; e < 4; ++e) {
              s1[i] = dot2(a[i][e], 0x3F803F80u, s1[i]);
              s2[i] = dot2(a[i][e], a[i][e], s2[i]);
            }
          }
        }
      }
#pragma unroll
      for (int i = 0; i < MB; ++i) asm volatile("" : "+v"(a[i]));
      wait_lgkm<(w0 > 15 ? 15 : w0)>();
#pragma unroll
      for (int j = 0; j < NB0; ++j) {
        asm volatile("" : "+v"(bl[KQ][j]), "+v"(bh[KQ][j]));
        u32x4 b = {bl[KQ][j][0], bl[KQ][j][1], bh[KQ][j][0], bh[KQ][j][1]};
#pragma unroll
        for (int i = 0; i < MB; ++i) acc[i][j] = mfma16(b, a[i], acc[i][j]);
      }
      wait_lgkm<(w1 > 15 ? 15 : w1)>();
#pragma unroll
      for (int j = NB0; j < NB; ++j) {
        asm volatile("" : "+v"(bl[KQ][j]), "+v"(bh[KQ][j]));
        u32x4 b = {bl[KQ][j][0], bl[KQ][j][1], bh[KQ][j][0], bh[KQ][j][1]};
#pragma unroll
        for (int i = 0; i < MB; ++i) acc[i][j] = mfma16(b, a[i], acc[i][j]);
      }
      if constexpr (LNA) {
        if (docs) {
#pragma unroll
          for (int j = 0; j < NB; ++j) {
            cs[j] = dot2(bl[KQ][j][0], 0x3F803F80u, cs[j]);
            cs[j] = dot2(bl[KQ][j][1], 0x3F803F80u, cs[j]);
            cs[j] = dot2(bh[KQ][j][0], 0x3F803F80u, cs[j]);
            cs[j] = dot2(bh[KQ][j][1], 0x3F803F80u, cs[j]);
          }
        }
      }
    });
    ++slot; if (slot == NS) slot = 0;
  }

  if constexpr (MODE != M_QKV) {
    constexpr int RSF = BN * 4 + 16;
    constexpr int PLB = BM * RSF;
    constexpr int O_ROWP = WK * PLB;
    constexpr int O_COLP = O_ROWP + WK * BM * 8;
    constexpr int O_ROWF = O_COLP + 8 * BN * 4;
    constexpr int O_COLF = O_ROWF + BM * 8;
    static_assert(O_COLF + BN * 4 <= RING, "epilogue LDS");
    constexpr int CH = BN / 8;
    constexpr int LPR = (CH <= 8) ? 8 : 16;
    constexpr int RPP = 512 / LPR;
    constexpr int NPASS = BM / RPP;
    const int ec = tid & (LPR - 1), er = tid / LPR;
    const bool eact = ec < CH;
    u32x4 eop[NPASS];
    if constexpr (MODE != M_WIN) {
      constexpr int LDE = (MODE == M_SP) ? F_ : D_;
#pragma unroll
      for (int ps = 0; ps < NPASS; ++ps)
        if (eact) eop[ps] = *(const u32x4*)(e0 + (size_t)(m0 + er + ps * RPP) * LDE + n0 + ec * 8);
    }
    wait_lgkm0();
    bar();
    {
      char* pl = smem + wk * PLB + (wm * (BM / WM) + r16) * RSF + (wn * (BN / WN) + 4 * g) * 4;
#pragma unroll
      for (int i = 0; i < MB; ++i)
#pragma unroll
        for (int j = 0; j < NB; ++j) *(f32x4*)(pl + i * 16 * RSF + j * 64) = acc[i][j];
    }
    if constexpr (MODE == M_WIN || MODE == M_SP) {
#pragma unroll
      for (int i = 0; i < MB; ++i) {
        if constexpr (MODE == M_WIN && WN == 2) {
          float a = red4(s1[i]);
          if (g == 0) *(float*)(smem + O_ROWP + (wk * BM + wm * (BM / WM) + i * 16 + r16) * 8 + wn * 4) = a;
        } else {
          float a = (MODE == M_WIN) ? red4(s1[i]) : red4(cc2[i][0] + cc2[i][1]);
          float b = (MODE == M_WIN) ? red4(s2[i]) : 0.f;
          if (g == 0) *(f32x2*)(smem + O_ROWP + (wk * BM + wm * (BM / WM) + i * 16 + r16) * 8) = (f32x2){a, b};
        }
      }
    }
    if constexpr (MODE == M_WIN) {
#pragma unroll
      for (int j = 0; j < NB; ++j) {
        float v = red4(cs[j]);
        if (g == 0) *(float*)(smem + O_COLP + ((wm * WK + wk) * BN + wn * (BN / WN) + j * 16 + r16) * 4) = v;
      }
    }
    wait_lgkm0();
    bar();
    if constexpr (MODE == M_WIN) {
      if (tid < BN) {
        float c = 0.f;
#pragma unroll
        for (int w = 0; w < WM * WK; ++w) c += *(const float*)(smem + O_COLP + (w * BN + tid) * 4);
        *(float*)(smem + O_COLF + tid * 4) = c;
      } else if (tid >= 128 && tid < 128 + BM) {
        int row = tid - 128;
        f32x2 t = {0.f, 0.f};
#pragma unroll
        for (int k = 0; k < WK; ++k) t += *(const f32x2*)(smem + O_ROWP + (k * BM + row) * 8);
        float mean = t[0] * (1.f / 512.f);
        float var = fmaxf(t[1] * (1.f / 512.f) - mean * mean, 0.f);
        *(f32x2*)(smem + O_ROWF + row * 8) = (f32x2){mean, rsqrtf(var + 1e-5f)};
      }
      wait_lgkm0();
      bar();
    }
    f32x4 cf0 = {0.f, 0.f, 0.f, 0.f}, cf1 = cf0;
    if constexpr (MODE == M_WIN) {
      if (eact) {
        cf0 = *(const f32x4*)(smem + O_COLF + ec * 32);
        cf1 = *(const f32x4*)(smem + O_COLF + ec * 32 + 16);
      }
    }
#pragma unroll
    for (int ps = 0; ps < NPASS; ++ps) {
      const int row = er + ps * RPP;
      const int tok = m0 + row;
      float st1 = 0.f, st2 = 0.f;
      if (eact) {
        f32x4 v0 = *(const f32x4*)(smem + row * RSF + ec * 32);
        f32x4 v1 = *(const f32x4*)(smem + row * RSF + ec * 32 + 16);
#pragma unroll
        for (int k = 1; k < WK; ++k) {
          v0 += *(const f32x4*)(smem + k * PLB + row * RSF + ec * 32);
          v1 += *(const f32x4*)(smem + k * PLB + row * RSF + ec * 32 + 16);
        }
        float v[8] = {v0[0], v0[1], v0[2], v0[3], v1[0], v1[1], v1[2], v1[3]};
        u32x4 ov;
        if constexpr (MODE == M_WO || MODE == M_OUT) {
#pragma unroll
          for (int e = 0; e < 4; ++e)
            ov[e] = pack2(bflo(eop[ps][e]) + rbf(v[2 * e]), bfhi(eop[ps][e]) + rbf(v[2 * e + 1]));
          *(u32x4*)(o0 + (size_t)tok * D_ + n0 + ec * 8) = ov;
        } else if constexpr (MODE == M_SP) {
          float c = 0.f;
#pragma unroll
          for (int k = 0; k < WK; ++k) c += *(const float*)(smem + O_ROWP + (k * BM + row) * 8);
          const float bias = bflo((unsigned)e1[tok]);
#pragma unroll
          for (int e = 0; e < 4; ++e) {
            float g0 = rbf(rbf(v[2 * e] - c) + bias), g1 = rbf(rbf(v[2 * e + 1] - c) + bias);
            ov[e] = pack2(bflo(eop[ps][e]) * g0, bfhi(eop[ps][e]) * g1);
          }
          *(u32x4*)(o0 + (size_t)tok * FH_ + n0 + ec * 8) = ov;
        } else {
          f32x2 ms = *(const f32x2*)(smem + O_ROWF + row * 8);
          float cf[8] = {cf0[0], cf0[1], cf0[2], cf0[3], cf1[0], cf1[1], cf1[2], cf1[3]};
          float zz[8];
#pragma unroll
          for (int e = 0; e < 8; ++e) {
            float x = rbf(ms[1] * (v[e] - ms[0] * cf[e]));
            float a = x * fmaf(x * x, -0.10294324f, -2.3022082f);
            float t = __builtin_amdgcn_exp2f(a);
            zz[e] = x * __builtin_amdgcn_rcpf(1.f + t);
          }
#pragma unroll
          for (int e = 0; e < 4; ++e) ov[e] = pack2(zz[2 * e], zz[2 * e + 1]);
          *(u32x4*)(o0 + (size_t)tok * F_ + n0 + ec * 8) = ov;
          if (n0 >= FH_) {
#pragma unroll
            for (int e = 0; e < 4; ++e) {
              st1 = dot2(ov[e], 0x3F803F80u, st1);
              st2 = dot2(ov[e], ov[e], st2);
            }
          }
        }
      }
      if constexpr (MODE == M_WIN) {
        if (n0 >= FH_) {
          static_assert(LPR == 16, "row reduce");
          st1 = rowsum16(st1);
          st2 = rowsum16(st2);
          if (ec == 15) *(f32x2*)(partout + (size_t)(((n0 - FH_) / BN) * 2048 + tok) * 2) = (f32x2){st1, st2};
        }
      }
    }
    return;
  }
  if constexpr (MODE == M_QKV) {
    static_assert(WK == 1 && WN == 2 && MB == 2, "qkv epilogue");
    wait_lgkm0();
    bar();
    float* csl = (float*)smem;
    f32x4* ex = (f32x4*)(smem + 4096);
#pragma unroll
    for (int j = 0; j < NB; ++j) {
      float v = red4(cs[j]);
      if (g == 0) csl[wid * (NB * 16) + j * 16 + r16] = v;
    }
    ex[wid * 64 + lane] = (f32x4){s1[0], 0.f, s1[1], 0.f};
    wait_lgkm0();
    bar();
    {
      f32x4 o = ex[(wid ^ 1) * 64 + lane];
      float m0_ = wn ? o[0] : s1[0], q0_ = wn ? s1[0] : o[0];
      float m1_ = wn ? o[2] : s1[1], q1_ = wn ? s1[1] : o[2];
      s1[0] = m0_; s2[0] = q0_;
      s1[1] = m1_; s2[1] = q1_;
    }
    float mean[MB], rstd[MB];
#pragma unroll
    for (int i = 0; i < MB; ++i) {
      float a = red4(s1[i]), b = red4(s2[i]);
      mean[i] = a * (1.f / 512.f);
      float var = fmaxf(b * (1.f / 512.f) - mean[i] * mean[i], 0.f);
      rstd[i] = rsqrtf(var + 1e-5f);
    }
    const int which = n0 >> 9;
#pragma unroll
    for (int j = 0; j < NB; ++j) {
      f32x4 c4 = {0.f, 0.f, 0.f, 0.f};
#pragma unroll
      for (int w = 0; w < 4; ++w) c4 += *(const f32x4*)(csl + (w * 2 + wn) * (NB * 16) + j * 16 + 4 * g);
      const int n = nbase + j * 16;
      const int head = (n & 511) >> 4;
      if (which < 2) {
        u16* dst = (which == 0 ? o0 : o1);
        const float sc = (which == 0) ? 0.36067376022224085f : 1.f;
#pragma unroll
        for (int i = 0; i < MB; ++i) {
          int tok = tokb + i * 16 + r16;
          float rs = rstd[i] * sc;
          float v0 = rs * (acc[i][j][0] - mean[i] * c4[0]);
          float v1 = rs * (acc[i][j][1] - mean[i] * c4[1]);
          float v2 = rs * (acc[i][j][2] - mean[i] * c4[2]);
          float v3 = rs * (acc[i][j][3] - mean[i] * c4[3]);
          *(u32x2*)(dst + ((size_t)head * S_ + tok) * 16 + 4 * g) = (u32x2){pack2(v0, v1), pack2(v2, v3)};
        }
      } else {
        u16* vt = (u16*)(smem + 16384 + (wid * NB + j) * (MB * 512));
#pragma unroll
        for (int i = 0; i < MB; ++i) {
#pragma unroll
          for (int e = 0; e < 4; ++e) {
            float v = rstd[i] * (acc[i][j][e] - mean[i] * c4[e]);
            __bf16 bv = (__bf16)v;
            vt[(4 * g + e) * (MB * 16) + i * 16 + r16] = __builtin_bit_cast(u16, bv);
          }
        }
        int d = lane >> 2, c = lane & 3;
        u32x4 v = *(const u32x4*)(vt + d * 32 + c * 8);
        *(u32x4*)(o2 + ((size_t)head * 16 + d) * S_ + tokb + c * 8) = v;
      }
    }
  }
}

constexpr int AT_KB = 384 * 32;
constexpr int AT_VS = 784;
constexpr int AT_VB = 32 * AT_VS;
constexpr int AT_HB = AT_KB + AT_VB;
__global__ __launch_bounds__(512, 2) void attn_k(const u16* __restrict__ Q, const u16* __restrict__ K,
                                                 const u16* __restrict__ Vt, u16* __restrict__ O2,
                                                 const u16* __restrict__ pf0, const u16* __restrict__ pf1) {
  __shared__ __attribute__((aligned(16))) char smem[2 * AT_HB];
  const int tid = threadIdx.x, lane = tid & 63;
  const int wid = __builtin_amdgcn_readfirstlane(tid >> 6);
  const int hp = blockIdx.x >> 4, qb = blockIdx.x & 15;
  const int hl = wid >> 2;
  const int head = hp * 2 + hl;
  const int qt = qb * 4 + (wid & 3);
  const int q0 = qt * 32;
  const int kw0 = qb * 128 - 256;
  const int i = lane & 31, hh = lane >> 5;

  {
    const int t = tid & 255;
    const u16* Kh = K + (size_t)head * S_ * 16;
    const u16* Vh = Vt + (size_t)head * 16 * S_;
    char* kl = smem + hl * AT_HB;
    char* vl = kl + AT_KB;
    u32x4 kv[3], vv[3];
#pragma unroll
    for (int r = 0; r < 3; ++r) {
      int c = t + 256 * r;
      int key = kw0 + (c >> 1); key = key < 0 ? 0 : key;
      kv[r] = *(const u32x4*)(Kh + key * 16 + (c & 1) * 8);
      int d = c / 48, cc = c % 48;
      int k2 = kw0 + cc * 8; k2 = k2 < 0 ? 0 : k2;
      vv[r] = *(const u32x4*)(Vh + d * S_ + k2);
    }
#pragma unroll
    for (int r = 0; r < 3; ++r) {
      int c = t + 256 * r;
      int d = c / 48, cc = c % 48;
      unsigned f = (d == 0) ? 0x3F803F80u : 0u;
      *(u32x4*)(vl + (16 + d) * AT_VS + cc * 16) = (u32x4){f, f, f, f};
    }
#pragma unroll
    for (int r = 0; r < 3; ++r) {
      int c = t + 256 * r;
      int row = c >> 1;
      *(u32x4*)(kl + row * 32 + (((c & 1) ^ ((row >> 3) & 1)) << 4)) = kv[r];
      int d = c / 48, cc = c % 48;
      *(u32x4*)(vl + d * AT_VS + cc * 16) = vv[r];
    }
  }
  u32x4 qf = *(const u32x4*)(Q + ((size_t)head * S_ + q0 + i) * 16 + hh * 8);

  const int t0 = (q0 >= 256) ? 0 : (8 - qt);
  const int pi = ((i >> 2) & 1) * 16 + ((i >> 4) & 1) * 8 + ((i >> 3) & 1) * 4 + (i & 3);
  const float NEG = -1e30f;
  f32x16 c0, c8, zero;
#pragma unroll
  for (int r = 0; r < 16; ++r) {
    int kap = 16 * hh + r;
    zero[r] = 0.f;
    c0[r] = (kap > i) ? 0.f : NEG;
    c8[r] = (kap <= i) ? 0.f : NEG;
  }
  __syncthreads();
  u32x4 pfv[5];
  {
    const u32x4* q0p = (const u32x4*)pf0;
    const int gi = blockIdx.x * 512 + tid;
#pragma unroll
    for (int r = 0; r < 4; ++r) pfv[r] = q0p[gi + r * 131072];
    pfv[4] = ((const u32x4*)pf1)[gi < 57344 ? gi : 0];
  }

  const int wrow = (wid & 3) * 32;
  const char* kr = smem + hl * AT_HB + (wrow + pi) * 32 + ((hh ^ ((pi >> 3) & 1)) << 4);
  const char* vr = smem + hl * AT_HB + AT_KB + i * AT_VS + (wrow + 16 * hh) * 2;

  u32x4 kf[9];
#pragma unroll
  for (int t = 0; t < 9; ++t) kf[t] = *(const u32x4*)(kr + t * 1024);

  float mx = NEG;
#pragma unroll
  for (int t = 0; t < 9; ++t) {
    if (t >= t0) {
      f32x16 s = mfma32(kf[t], qf, t == 0 ? c0 : (t == 8 ? c8 : zero));
#pragma unroll
      for (int r = 0; r < 16; r += 2) mx = fmaxf(fmaxf(mx, s[r]), s[r + 1]);
    }
  }
  {
    unsigned u = __float_as_uint(mx);
    auto r = __builtin_amdgcn_permlane32_swap(u, u, false, false);
    mx = fmaxf(__uint_as_float(r[0]), __uint_as_float(r[1]));
  }
  f32x16 cm;
#pragma unroll
  for (int r = 0; r < 16; ++r) { cm[r] = -mx; c0[r] -= mx; c8[r] -= mx; }

  f32x16 oacc = zero;
#pragma unroll
  for (int t = 0; t < 9; ++t) {
    if (t >= t0) {
      u32x4 v0 = *(const u32x4*)(vr + t * 64);
      u32x4 v1 = *(const u32x4*)(vr + t * 64 + 16);
      f32x16 s = mfma32(kf[t], qf, t == 0 ? c0 : (t == 8 ? c8 : cm));
      float p[16];
#pragma unroll
      for (int r = 0; r < 16; ++r) p[r] = __builtin_amdgcn_exp2f(s[r]);
      u32x4 p0 = {pack2(p[0], p[1]), pack2(p[2], p[3]), pack2(p[4], p[5]), pack2(p[6], p[7])};
      u32x4 p1 = {pack2(p[8], p[9]), pack2(p[10], p[11]), pack2(p[12], p[13]), pack2(p[14], p[15])};
      oacc = mfma32(v0, p0, oacc);
      oacc = mfma32(v1, p1, oacc);
    }
  }
  float l;
  {
    unsigned u = __float_as_uint(oacc[8]);
    auto r = __builtin_amdgcn_permlane32_swap(u, u, false, false);
    l = __uint_as_float(r[0]) + __uint_as_float(r[1]);
  }
  float inv = 1.f / l;
  __syncthreads();
  {
    char* ot = smem + ((wid & 3) * 32 + i) * 64 + hl * 32 + hh * 8;
    *(u32x2*)(ot) = (u32x2){pack2(oacc[0] * inv, oacc[1] * inv), pack2(oacc[2] * inv, oacc[3] * inv)};
    *(u32x2*)(ot + 16) = (u32x2){pack2(oacc[4] * inv, oacc[5] * inv), pack2(oacc[6] * inv, oacc[7] * inv)};
  }
  __syncthreads();
  *(u32x4*)(O2 + ((size_t)hp * S_ + qb * 128) * 32 + tid * 8) = *(const u32x4*)(smem + tid * 16);
  asm volatile("" ::"v"(pfv[0]), "v"(pfv[1]), "v"(pfv[2]), "v"(pfv[3]), "v"(pfv[4]));
}

extern "C" void kernel_launch(void* const* d_in, const int* in_sizes, int n_in, void* d_out, int out_size,
                              void* d_ws, size_t ws_size, hipStream_t stream) {
  const u16* x = (const u16*)d_in[0];
  const u16* wqkv = (const u16*)d_in[1];
  const u16* wo = (const u16*)d_in[2];
  const u16* w_in = (const u16*)d_in[4];
  const u16* w_sp = (const u16*)d_in[5];
  const u16* b_sp = (const u16*)d_in[6];
  const u16* w_out = (const u16*)d_in[7];
  u16* out = (u16*)d_out;

  char* ws = (char*)d_ws;
  u16* Qb = (u16*)(ws);
  u16* Kb = (u16*)(ws + (2u << 20));
  u16* Vb = (u16*)(ws + (4u << 20));
  u16* Ob = (u16*)(ws + (6u << 20));
  u16* Hb = (u16*)(ws + (8u << 20));
  u16* Zb = (u16*)(ws + (10u << 20));
  u16* Ub = (u16*)(ws + (18u << 20));
  float* Pb = (float*)(ws + (22u << 20));

  gemm_k<128, 128, 4, 2, 1, 1, 8, 4, M_QKV, 512, 1536, 4, 6><<<256, 768, 0, stream>>>(
      x, wqkv, wo, w_in, nullptr, Qb, Kb, Vb, nullptr);
  attn_k<<<256, 512, 0, stream>>>(Qb, Kb, Vb, Ob, w_sp, w_out);
  gemm_k<64, 64, 4, 2, 1, 1, 8, 8, M_WO, 512, 512, 4, 8><<<256, 768, 0, stream>>>(
      Ob, wo, x, nullptr, nullptr, Hb, nullptr, nullptr, nullptr);
  gemm_k<128, 128, 4, 2, 1, 1, 8, 4, M_WIN, 512, 1792, 4, 7><<<224, 768, 0, stream>>>(
      Hb, w_in, nullptr, nullptr, nullptr, Zb, nullptr, nullptr, Pb);
  gemm_k<64, 128, 2, 1, 4, 2, 16, 3, M_SP, 2048, 1792, 4, 7><<<224, 768, 0, stream>>>(
      w_sp, Zb + FH_, Zb, b_sp, Pb, Ub, nullptr, nullptr, nullptr);
  gemm_k<64, 64, 4, 2, 1, 1, 14, 8, M_OUT, 896, 512, 4, 8><<<256, 768, 0, stream>>>(
      Ub, w_out, Hb, nullptr, nullptr, out, nullptr, nullptr, nullptr);
}
```

```cpp
#include <hip/hip_runtime.h>
#include <hip/hip_bf16.h>
#include <type_traits>

typedef unsigned short u16;
typedef short s16x8 __attribute__((ext_vector_type(8)));
typedef short s16x4 __attribute__((ext_vector_type(4)));
typedef float f32x4 __attribute__((ext_vector_type(4)));
typedef float f32x2 __attribute__((ext_vector_type(2)));
typedef float f32x16 __attribute__((ext_vector_type(16)));
typedef __bf16 bf16x2 __attribute__((ext_vector_type(2)));
typedef __bf16 bf16x8 __attribute__((ext_vector_type(8)));
typedef unsigned u32x4 __attribute__((ext_vector_type(4)));
typedef unsigned u32x2 __attribute__((ext_vector_type(2)));

#define DI __device__ __forceinline__
#define SCHED_FENCE __builtin_amdgcn_sched_barrier(0)

constexpr int S_ = 2048, D_ = 512, H_ = 32, DH_ = 16, F_ = 1792, FH_ = 896;

DI unsigned pack2(float a, float b) {
  bf16x2 v = {(__bf16)a, (__bf16)b};
  return __builtin_bit_cast(unsigned, v);
}
DI float bflo(unsigned u) { return __uint_as_float(u << 16); }
DI float bfhi(unsigned u) { return __uint_as_float(u & 0xffff0000u); }
DI float rbf(float a) {
  __bf16 v = (__bf16)a;
  return __uint_as_float(((unsigned)__builtin_bit_cast(u16, v)) << 16);
}
DI float red4(float v) {
  unsigned u = __float_as_uint(v);
  auto r = __builtin_amdgcn_permlane16_swap(u, u, false, false);
  float a = __uint_as_float(r[0]) + __uint_as_float(r[1]);
  unsigned w = __float_as_uint(a);
  auto q = __builtin_amdgcn_permlane32_swap(w, w, false, false);
  return __uint_as_float(q[0]) + __uint_as_float(q[1]);
}
DI float rowsum16(float v) {
  v += __int_as_float(__builtin_amdgcn_update_dpp(0, __float_as_int(v), 0x111, 0xf, 0xf, true));
  v += __int_as_float(__builtin_amdgcn_update_dpp(0, __float_as_int(v), 0x112, 0xf, 0xf, true));
  v += __int_as_float(__builtin_amdgcn_update_dpp(0, __float_as_int(v), 0x114, 0xf, 0xf, true));
  v += __int_as_float(__builtin_amdgcn_update_dpp(0, __float_as_int(v), 0x118, 0xf, 0xf, true));
  return v;
}
DI float dot2(unsigned a, unsigned b, float c) {
  return __builtin_amdgcn_fdot2_f32_bf16(__builtin_bit_cast(bf16x2, a), __builtin_bit_cast(bf16x2, b), c, false);
}
DI f32x4 mfma16(u32x4 a, u32x4 b, f32x4 c) {
  return __builtin_amdgcn_mfma_f32_16x16x32_bf16(__builtin_bit_cast(bf16x8, a), __builtin_bit_cast(bf16x8, b), c, 0, 0, 0);
}
DI f32x16 mfma32(u32x4 a, u32x4 b, f32x16 c) {
  return __builtin_amdgcn_mfma_f32_32x32x16_bf16(__builtin_bit_cast(bf16x8, a), __builtin_bit_cast(bf16x8, b), c, 0, 0, 0);
}
template <int N> DI void wait_vm() { asm volatile("s_waitcnt vmcnt(%0)" ::"n"(N) : "memory"); }
DI void wait_lgkm0() { asm volatile("s_waitcnt lgkmcnt(0)" ::: "memory"); }
DI void bar() {
  asm volatile("" ::: "memory");
  __builtin_amdgcn_s_barrier();
  asm volatile("" ::: "memory");
}

template <int OFF> DI u32x2 tr_read(unsigned addr) {
  u32x2 r;
  asm volatile("ds_read_b64_tr_b16 %0, %1 offset:%2" : "=v"(r) : "v"(addr), "n"(OFF));
  return r;
}
template <int OFF> DI u32x4 lds_read128(unsigned addr) {
  u32x4 r;
  asm volatile("ds_read_b128 %0, %1 offset:%2" : "=v"(r) : "v"(addr), "n"(OFF));
  return r;
}
template <int N> DI void wait_lgkm() { asm volatile("s_waitcnt lgkmcnt(%0)" ::"n"(N) : "memory"); }
template <int I, int N, class F> DI void static_for(F&& f) {
  if constexpr (I < N) {
    f(std::integral_constant<int, I>{});
    static_for<I + 1, N>(f);
  }
}
DI void glds16(const void* g, void* l) {
  __builtin_amdgcn_global_load_lds((const unsigned*)g, (unsigned*)l, 16, 0, 0);
}

template <int KT, int NS> constexpr int iss_before(int kt) {
  int is = 2;
  for (int k = 0; k < kt; ++k) {
    int n = is + 2;
    if (n > k + NS) n = k + NS;
    if (n > KT) n = KT;
    is = n;
  }
  return is;
}
enum { M_QKV = 0, M_WO = 1, M_WIN = 2, M_SP = 3, M_OUT = 4 };

template <int BM, int BN, int WM, int WN, int WK, int SUB, int KT, int NS, int MODE, int LDA, int LDB, int PM, int PN>
__global__ __launch_bounds__(768, 3) void gemm_k(const u16* __restrict__ A, const u16* __restrict__ B,
                                                 const u16* __restrict__ e0, const u16* __restrict__ e1,
                                                 const float* __restrict__ partin, u16* __restrict__ o0,
                                                 u16* __restrict__ o1, u16* __restrict__ o2,
                                                 float* __restrict__ partout) {
  static_assert(WM * WN * WK == 8, "8 consumer waves");
  constexpr int AB = BM * 128, BB = BN * 128, SUBB = AB + BB, STG = SUB * SUBB;
  constexpr int AI = AB / 1024, BI = BB / 1024;
  static_assert(AI % 4 == 0, "A instr");
  constexpr int LA = AI / 4;
  constexpr int LBF = BI / 4, LBR = BI % 4;
  constexpr int MB = BM / WM / 16, NB = BN / WN / 16;
  constexpr int RING = NS * STG;
  constexpr int EXTRA = (MODE == M_SP) ? 16384 : 0;
  constexpr bool LNA = (MODE == M_QKV || MODE == M_WIN);
  __shared__ __attribute__((aligned(16))) char smem[RING + EXTRA];

  const int tid = threadIdx.x, lane = tid & 63;
  const int wid = __builtin_amdgcn_readfirstlane(tid >> 6);

  if constexpr (MODE == M_QKV) {
    if (blockIdx.x >= 192) {
      const int pb = blockIdx.x - 192;
      u32x4 accx = {0u, 0u, 0u, 0u};
      auto pf = [&](const u16* p, int n16) {
        const u32x4* q = (const u32x4*)p;
        for (int i = pb * 768 + tid; i < n16; i += 64 * 768 * 4) {
          u32x4 a = q[i];
          u32x4 b = (i + 64 * 768 < n16) ? q[i + 64 * 768] : a;
          u32x4 c = (i + 2 * 64 * 768 < n16) ? q[i + 2 * 64 * 768] : a;
          u32x4 d = (i + 3 * 64 * 768 < n16) ? q[i + 3 * 64 * 768] : a;
          accx ^= a ^ b ^ c ^ d;
        }
      };
      pf(e0, 512 * 512 / 8);
      pf(e1, 512 * 1792 / 8);
      asm volatile("" ::"v"(accx));
      return;
    }
  }
  constexpr int TN = (MODE == M_QKV ? 1536 : MODE == M_WIN ? 1792 : MODE == M_SP ? 896 : 512) / BN;
  constexpr int PXN = TN / PN;
  const int bid = blockIdx.x, xcd = bid & 7, jj = bid >> 3;
  const int tm = (xcd / PXN) * PM + jj / PN, tn = (xcd % PXN) * PN + jj % PN;
  const int m0 = tm * BM, n0 = tn * BN;

  if (wid >= 8) {
    const int lw = wid - 8;
    unsigned aoff[LA], boff[LBF + 1];
#pragma unroll
    for (int i = 0; i < LA; ++i) {
      int q = lw + 4 * i;
      int row = q * 8 + (lane >> 3), pos = lane & 7;
      int ch = pos ^ (row & 7);
      if constexpr (MODE == M_WO) aoff[i] = (unsigned)(((ch >> 2) * S_ + m0 + row) * 32 + (ch & 3) * 8);
      else aoff[i] = (unsigned)((m0 + row) * LDA + (ch << 3));
    }
#pragma unroll
    for (int i = 0; i < LBF; ++i) {
      int q = lw + 4 * i;
      int r = (q & 7) * 8 + (lane >> 3), pos = lane & 7;
      int h = ((r >> 1) & 1) | (((r >> 3) & 1) << 1);
      int ch = (((pos >> 1) ^ h) << 1) | (pos & 1);
      boff[i] = (unsigned)(r * LDB + n0 + (q >> 3) * 64 + ch * 8);
    }
    static_assert(LBR == 0, "BN % 32");
    const bool extraB = (lw < LBR);
    auto issue = [&](int kt, int slot) {
#pragma unroll
      for (int sb = 0; sb < SUB; ++sb) {
        const u16* a = A + (kt * SUB + sb) * (MODE == M_WO ? 2 * S_ * 32 : 64);
        const u16* b = B + (size_t)(kt * SUB + sb) * 64 * LDB;
        char* s = smem + slot * STG + sb * SUBB;
#pragma unroll
        for (int i = 0; i < LA; ++i) glds16(a + aoff[i], s + (lw + 4 * i) * 1024);
#pragma unroll
        for (int i = 0; i < LBF; ++i) glds16(b + boff[i], s + AB + (lw + 4 * i) * 1024);
        if (LBR != 0) {
          if (extraB) glds16(b + boff[LBF], s + AB + (lw + 4 * LBF) * 1024);
        }
      }
    };
    issue(0, 0);
    issue(1, 1);
    static_for<0, KT>([&](auto KTI) {
      constexpr int kt = KTI;
      constexpr int is0 = iss_before<KT, NS>(kt);
      constexpr int is1 = iss_before<KT, NS>(kt + 1);
      constexpr int later = is0 - 1 - kt;
      if (LBR != 0 && extraB) wait_vm<later * SUB * (LA + LBF + 1)>();
      else wait_vm<later * SUB * (LA + LBF)>();
      bar();
      if constexpr (is1 > is0) issue(is0, is0 % NS);
      if constexpr (is1 > is0 + 1) issue(is0 + 1, (is0 + 1) % NS);
    });
    return;
  }

  const int wk = wid % WK;
  const int wmn = wid / WK;
  const int wm = wmn / WN, wn = wmn % WN;
  const int r16 = lane & 15, g = lane >> 4;
  const int tokb = m0 + wm * (BM / WM);
  const int nbase = n0 + wn * (BN / WN);

  if constexpr (MODE == M_SP) {
    float* sr = (float*)(smem + RING);
    float* srm = sr + 2048;
#pragma unroll
    for (int j = 0; j < 2; ++j) {
      int row = 2 * tid + 1024 * j;
      f32x4 t = {0.f, 0.f, 0.f, 0.f};
#pragma unroll
      for (int p = 0; p < 7; ++p) {
        f32x4 v = *(const f32x4*)(partin + (size_t)(p * 2048 + row) * 2);
        t += v;
      }
      float mean0 = t[0] * (1.f / 896.f), mean1 = t[2] * (1.f / 896.f);
      float var0 = fmaxf(t[1] * (1.f / 896.f) - mean0 * mean0, 0.f);
      float var1 = fmaxf(t[3] * (1.f / 896.f) - mean1 * mean1, 0.f);
      float r0 = rsqrtf(var0 + 1e-5f), r1 = rsqrtf(var1 + 1e-5f);
      *(f32x2*)(sr + row) = (f32x2){r0, r1};
      *(f32x2*)(srm + row) = (f32x2){r0 * mean0, r1 * mean1};
    }
  }

  f32x4 acc[MB][NB];
#pragma unroll
  for (int i = 0; i < MB; ++i)
#pragma unroll
    for (int j = 0; j < NB; ++j) acc[i][j] = (f32x4){0.f, 0.f, 0.f, 0.f};
  float s1[MB], s2[MB], cs[NB];
  f32x2 cc2[MB];
#pragma unroll
  for (int i = 0; i < MB; ++i) { s1[i] = 0.f; s2[i] = 0.f; cc2[i] = (f32x2){0.f, 0.f}; }
#pragma unroll
  for (int j = 0; j < NB; ++j) cs[j] = 0.f;

  constexpr int NKS = 2 * SUB / WK;
  static_assert(NKS >= 1 && NKS <= 2, "NKS");
  static_assert(NKS == 1 || WK <= 2, "NKS");
  const unsigned lbase = (unsigned)(size_t)smem;
  unsigned ard[NKS], bpo[4];
  {
    int q = (lane & 15) >> 2, p = lane & 3;
    int row = wm * (BM / WM) + r16;
#pragma unroll
    for (int kq = 0; kq < NKS; ++kq) {
      int kk = wk + WK * kq;
      int sub = kk >> 1, ks = kk & 1;
      ard[kq] = lbase + sub * SUBB + row * 128 + (((4 * ks + g) ^ (row & 7)) << 4);
    }
    int h = ((q >> 1) & 1) | ((g & 1) << 1);
    int kk0 = wk;
    unsigned bb = lbase + (kk0 >> 1) * SUBB + AB + ((kk0 & 1) * 32 + 8 * g + q) * 128 + p * 8;
    constexpr int NBW = BN / WN / 16;
    static_assert(NBW % 4 == 0 || NBW == 2, "wave n-blocks");
    int nb0 = wn * NBW;
#pragma unroll
    for (int pc = 0; pc < 4; ++pc) {
      int nb = nb0 + pc;
      bpo[pc] = bb + (nb >> 2) * 8192 + (((nb & 3) ^ h) << 5);
    }
  }
  unsigned strd = lbase + RING + ((wk & 1) * 32 + (wk >> 1) * 64 + g * 8) * 4;

  constexpr int ST = (MODE == M_SP) ? 4 : 0;
  constexpr int PER = ST + MB + 2 * NB;
  constexpr int RTOT = NKS * PER;
  constexpr int NB0 = (NB + 1) / 2;

  int slot = 0;
#pragma unroll 1
  for (int kt = 0; kt < KT; ++kt) {
    wait_lgkm0();
    bar();
    const unsigned so = slot * STG;
    u32x4 af[NKS][MB];
    u32x2 bl[NKS][NB], bh[NKS][NB];
    u32x4 st[4];
    static_for<0, NKS>([&](auto KQ) {
      if constexpr (MODE == M_SP) {
        st[0] = lds_read128<0>(strd);
        st[1] = lds_read128<16>(strd);
        st[2] = lds_read128<8192>(strd);
        st[3] = lds_read128<8208>(strd);
      }
      static_for<0, MB>([&](auto I) { af[KQ][I] = lds_read128<I * 2048>(ard[KQ] + so); });
      static_for<0, NB>([&](auto J) {
        constexpr int off = KQ * (WK == 1 ? 4096 : SUBB) + (J >> 2) * 8192;
        bl[KQ][J] = tr_read<off>(bpo[J & 3] + so);
        bh[KQ][J] = tr_read<off + 512>(bpo[J & 3] + so);
      });
    });
    if constexpr (MODE == M_SP) strd += SUB * 256;
    const bool docs = LNA && ((kt % WM) == wm);
    static_for<0, NKS>([&](auto KQ) {
      constexpr int w0 = RTOT - (KQ * PER + ST + MB + 2 * NB0);
      constexpr int w1 = RTOT - (KQ + 1) * PER;
      constexpr int wa = RTOT - (KQ * PER + ST + MB);
      wait_lgkm<(wa > 15 ? 15 : wa)>();
      u32x4 a[MB];
#pragma unroll
      for (int i = 0; i < MB; ++i) {
        asm volatile("" : "+v"(af[KQ][i]));
        a[i] = af[KQ][i];
      }
      if constexpr (MODE == M_SP) {
        asm volatile("" : "+v"(st[0]), "+v"(st[1]), "+v"(st[2]), "+v"(st[3]));
        f32x4 r0 = __builtin_bit_cast(f32x4, st[0]), r1 = __builtin_bit_cast(f32x4, st[1]);
        f32x4 m0v = __builtin_bit_cast(f32x4, st[2]), m1v = __builtin_bit_cast(f32x4, st[3]);
#pragma unroll
        for (int i = 0; i < MB; ++i) {
          f32x2 w01 = {bflo(a[i][0]), bfhi(a[i][0])}, w23 = {bflo(a[i][1]), bfhi(a[i][1])};
          f32x2 w45 = {bflo(a[i][2]), bfhi(a[i][2])}, w67 = {bflo(a[i][3]), bfhi(a[i][3])};
          f32x2 c = cc2[i];
          c = __builtin_elementwise_fma(w01, (f32x2){m0v[0], m0v[1]}, c);
          c = __builtin_elementwise_fma(w23, (f32x2){m0v[2], m0v[3]}, c);
          c = __builtin_elementwise_fma(w45, (f32x2){m1v[0], m1v[1]}, c);
          c = __builtin_elementwise_fma(w67, (f32x2){m1v[2], m1v[3]}, c);
          cc2[i] = c;
          w01 *= (f32x2){r0[0], r0[1]}; w23 *= (f32x2){r0[2], r0[3]};
          w45 *= (f32x2){r1[0], r1[1]}; w67 *= (f32x2){r1[2], r1[3]};
          a[i][0] = pack2(w01[0], w01[1]);
          a[i][1] = pack2(w23[0], w23[1]);
          a[i][2] = pack2(w45[0], w45[1]);
          a[i][3] = pack2(w67[0], w67[1]);
        }
      }
      if constexpr (LNA) {
#pragma unroll
        for (int i = 0; i < MB; ++i) {
          if constexpr (WN == 2) {
#pragma unroll
            for (int e = 0; e < 4; ++e) s1[i] = dot2(a[i][e], wn ? a[i][e] : 0x3F803F80u, s1[i]);
          } else {
#pragma unroll
            for (int e = 0; e < 4; ++e) {
              s1[i] = dot2(a[i][e], 0x3F803F80u, s1[i]);
              s2[i] = dot2(a[i][e], a[i][e], s2[i]);
            }
          }
        }
      }
#pragma unroll
      for (int i = 0; i < MB; ++i) asm volatile("" : "+v"(a[i]));
      wait_lgkm<(w0 > 15 ? 15 : w0)>();
#pragma unroll
      for (int j = 0; j < NB0; ++j) {
        asm volatile("" : "+v"(bl[KQ][j]), "+v"(bh[KQ][j]));
        u32x4 b = {bl[KQ][j][0], bl[KQ][j][1], bh[KQ][j][0], bh[KQ][j][1]};
#pragma unroll
        for (int i = 0; i < MB; ++i) acc[i][j] = mfma16(b, a[i], acc[i][j]);
      }
      wait_lgkm<(w1 > 15 ? 15 : w1)>();
#pragma unroll
      for (int j = NB0; j < NB; ++j) {
        asm volatile("" : "+v"(bl[KQ][j]), "+v"(bh[KQ][j]));
        u32x4 b = {bl[KQ][j][0], bl[KQ][j][1], bh[KQ][j][0], bh[KQ][j][1]};
#pragma unroll
        for (int i = 0; i < MB; ++i) acc[i][j] = mfma16(b, a[i], acc[i][j]);
      }
      if constexpr (LNA) {
        if (docs) {
#pragma unroll
          for (int j = 0; j < NB; ++j) {
            cs[j] = dot2(bl[KQ][j][0], 0x3F803F80u, cs[j]);
            cs[j] = dot2(bl[KQ][j][1], 0x3F803F80u, cs[j]);
            cs[j] = dot2(bh[KQ][j][0], 0x3F803F80u, cs[j]);
            cs[j] = dot2(bh[KQ][j][1], 0x3F803F80u, cs[j]);
          }
        }
      }
    });
    ++slot; if (slot == NS) slot = 0;
  }

  if constexpr (MODE != M_QKV) {
    constexpr int RSF = BN * 4 + 16;
    constexpr int PLB = BM * RSF;
    constexpr int O_ROWP = WK * PLB;
    constexpr int O_COLP = O_ROWP + WK * BM * 8;
    constexpr int O_ROWF = O_COLP + 8 * BN * 4;
    constexpr int O_COLF = O_ROWF + BM * 8;
    static_assert(O_COLF + BN * 4 <= RING, "epilogue LDS");
    constexpr int CH = BN / 8;
    constexpr int LPR = (CH <= 8) ? 8 : 16;
    constexpr int RPP = 512 / LPR;
    constexpr int NPASS = BM / RPP;
    const int ec = tid & (LPR - 1), er = tid / LPR;
    const bool eact = ec < CH;
    u32x4 eop[NPASS];
    if constexpr (MODE != M_WIN) {
      constexpr int LDE = (MODE == M_SP) ? F_ : D_;
#pragma unroll
      for (int ps = 0; ps < NPASS; ++ps)
        if (eact) eop[ps] = *(const u32x4*)(e0 + (size_t)(m0 + er + ps * RPP) * LDE + n0 + ec * 8);
    }
    wait_lgkm0();
    bar();
    {
      char* pl = smem + wk * PLB + (wm * (BM / WM) + r16) * RSF + (wn * (BN / WN) + 4 * g) * 4;
#pragma unroll
      for (int i = 0; i < MB; ++i)
#pragma unroll
        for (int j = 0; j < NB; ++j) *(f32x4*)(pl + i * 16 * RSF + j * 64) = acc[i][j];
    }
    if constexpr (MODE == M_WIN || MODE == M_SP) {
#pragma unroll
      for (int i = 0; i < MB; ++i) {
        if constexpr (MODE == M_WIN && WN == 2) {
          float a = red4(s1[i]);
          if (g == 0) *(float*)(smem + O_ROWP + (wk * BM + wm * (BM / WM) + i * 16 + r16) * 8 + wn * 4) = a;
        } else {
          float a = (MODE == M_WIN) ? red4(s1[i]) : red4(cc2[i][0] + cc2[i][1]);
          float b = (MODE == M_WIN) ? red4(s2[i]) : 0.f;
          if (g == 0) *(f32x2*)(smem + O_ROWP + (wk * BM + wm * (BM / WM) + i * 16 + r16) * 8) = (f32x2){a, b};
        }
      }
    }
    if constexpr (MODE == M_WIN) {
#pragma unroll
      for (int j = 0; j < NB; ++j) {
        float v = red4(cs[j]);
        if (g == 0) *(float*)(smem + O_COLP + ((wm * WK + wk) * BN + wn * (BN / WN) + j * 16 + r16) * 4) = v;
      }
    }
    wait_lgkm0();
    bar();
    if constexpr (MODE == M_WIN) {
      if (tid < BN) {
        float c = 0.f;
#pragma unroll
        for (int w = 0; w < WM * WK; ++w) c += *(const float*)(smem + O_COLP + (w * BN + tid) * 4);
        *(float*)(smem + O_COLF + tid * 4) = c;
      } else if (tid >= 128 && tid < 128 + BM) {
        int row = tid - 128;
        f32x2 t = {0.f, 0.f};
#pragma unroll
        for (int k = 0; k < WK; ++k) t += *(const f32x2*)(smem + O_ROWP + (k * BM + row) * 8);
        float mean = t[0] * (1.f / 512.f);
        float var = fmaxf(t[1] * (1.f / 512.f) - mean * mean, 0.f);
        *(f32x2*)(smem + O_ROWF + row * 8) = (f32x2){mean, rsqrtf(var + 1e-5f)};
      }
      wait_lgkm0();
      bar();
    }
    f32x4 cf0 = {0.f, 0.f, 0.f, 0.f}, cf1 = cf0;
    if constexpr (MODE == M_WIN) {
      if (eact) {
        cf0 = *(const f32x4*)(smem + O_COLF + ec * 32);
        cf1 = *(const f32x4*)(smem + O_COLF + ec * 32 + 16);
      }
    }
#pragma unroll
    for (int ps = 0; ps < NPASS; ++ps) {
      const int row = er + ps * RPP;
      const int tok = m0 + row;
      float st1 = 0.f, st2 = 0.f;
      if (eact) {
        f32x4 v0 = *(const f32x4*)(smem + row * RSF + ec * 32);
        f32x4 v1 = *(const f32x4*)(smem + row * RSF + ec * 32 + 16);
#pragma unroll
        for (int k = 1; k < WK; ++k) {
          v0 += *(const f32x4*)(smem + k * PLB + row * RSF + ec * 32);
          v1 += *(const f32x4*)(smem + k * PLB + row * RSF + ec * 32 + 16);
        }
        float v[8] = {v0[0], v0[1], v0[2], v0[3], v1[0], v1[1], v1[2], v1[3]};
        u32x4 ov;
        if constexpr (MODE == M_WO || MODE == M_OUT) {
#pragma unroll
          for (int e = 0; e < 4; ++e)
            ov[e] = pack2(bflo(eop[ps][e]) + rbf(v[2 * e]), bfhi(eop[ps][e]) + rbf(v[2 * e + 1]));
          *(u32x4*)(o0 + (size_t)tok * D_ + n0 + ec * 8) = ov;
        } else if constexpr (MODE == M_SP) {
          float c = 0.f;
#pragma unroll
          for (int k = 0; k < WK; ++k) c += *(const float*)(smem + O_ROWP + (k * BM + row) * 8);
          const float bias = bflo((unsigned)e1[tok]);
#pragma unroll
          for (int e = 0; e < 4; ++e) {
            float g0 = rbf(rbf(v[2 * e] - c) + bias), g1 = rbf(rbf(v[2 * e + 1] - c) + bias);
            ov[e] = pack2(bflo(eop[ps][e]) * g0, bfhi(eop[ps][e]) * g1);
          }
          *(u32x4*)(o0 + (size_t)tok * FH_ + n0 + ec * 8) = ov;
        } else {
          f32x2 ms = *(const f32x2*)(smem + O_ROWF + row * 8);
          float cf[8] = {cf0[0], cf0[1], cf0[2], cf0[3], cf1[0], cf1[1], cf1[2], cf1[3]};
          float zz[8];
#pragma unroll
          for (int e = 0; e < 8; ++e) {
            float x = rbf(ms[1] * (v[e] - ms[0] * cf[e]));
            float a = x * fmaf(x * x, -0.10294324f, -2.3022082f);
            float t = __builtin_amdgcn_exp2f(a);
            zz[e] = x * __builtin_amdgcn_rcpf(1.f + t);
          }
#pragma unroll
          for (int e = 0; e < 4; ++e) ov[e] = pack2(zz[2 * e], zz[2 * e + 1]);
          *(u32x4*)(o0 + (size_t)tok * F_ + n0 + ec * 8) = ov;
          if (n0 >= FH_) {
#pragma unroll
            for (int e = 0; e < 4; ++e) {
              st1 = dot2(ov[e], 0x3F803F80u, st1);
              st2 = dot2(ov[e], ov[e], st2);
            }
          }
        }
      }
      if constexpr (MODE == M_WIN) {
        if (n0 >= FH_) {
          static_assert(LPR == 16, "row reduce");
          st1 = rowsum16(st1);
          st2 = rowsum16(st2);
          if (ec == 15) *(f32x2*)(partout + (size_t)(((n0 - FH_) / BN) * 2048 + tok) * 2) = (f32x2){st1, st2};
        }
      }
    }
    return;
  }
  if constexpr (MODE == M_QKV) {
    static_assert(WK == 1 && WN == 2 && MB == 2, "qkv epilogue");
    wait_lgkm0();
    bar();
    float* csl = (float*)smem;
    f32x4* ex = (f32x4*)(smem + 4096);
#pragma unroll
    for (int j = 0; j < NB; ++j) {
      float v = red4(cs[j]);
      if (g == 0) csl[wid * (NB * 16) + j * 16 + r16] = v;
    }
    ex[wid * 64 + lane] = (f32x4){s1[0], 0.f, s1[1], 0.f};
    wait_lgkm0();
    bar();
    {
      f32x4 o = ex[(wid ^ 1) * 64 + lane];
      float m0_ = wn ? o[0] : s1[0], q0_ = wn ? s1[0] : o[0];
      float m1_ = wn ? o[2] : s1[1], q1_ = wn ? s1[1] : o[2];
      s1[0] = m0_; s2[0] = q0_;
      s1[1] = m1_; s2[1] = q1_;
    }
    float mean[MB], rstd[MB];
#pragma unroll
    for (int i = 0; i < MB; ++i) {
      float a = red4(s1[i]), b = red4(s2[i]);
      mean[i] = a * (1.f / 512.f);
      float var = fmaxf(b * (1.f / 512.f) - mean[i] * mean[i], 0.f);
      rstd[i] = rsqrtf(var + 1e-5f);
    }
    const int which = n0 >> 9;
#pragma unroll
    for (int j = 0; j < NB; ++j) {
      f32x4 c4 = {0.f, 0.f, 0.f, 0.f};
#pragma unroll
      for (int w = 0; w < 4; ++w) c4 += *(const f32x4*)(csl + (w * 2 + wn) * (NB * 16) + j * 16 + 4 * g);
      const int n = nbase + j * 16;
      const int head = (n & 511) >> 4;
      if (which < 2) {
        u16* dst = (which == 0 ? o0 : o1);
        const float sc = (which == 0) ? 0.36067376022224085f : 1.f;
#pragma unroll
        for (int i = 0; i < MB; ++i) {
          int tok = tokb + i * 16 + r16;
          float rs = rstd[i] * sc;
          float v0 = rs * (acc[i][j][0] - mean[i] * c4[0]);
          float v1 = rs * (acc[i][j][1] - mean[i] * c4[1]);
          float v2 = rs * (acc[i][j][2] - mean[i] * c4[2]);
          float v3 = rs * (acc[i][j][3] - mean[i] * c4[3]);
          *(u32x2*)(dst + ((size_t)head * S_ + tok) * 16 + 4 * g) = (u32x2){pack2(v0, v1), pack2(v2, v3)};
        }
      } else {
        u16* vt = (u16*)(smem + 16384 + (wid * NB + j) * (MB * 512));
#pragma unroll
        for (int i = 0; i < MB; ++i) {
#pragma unroll
          for (int e = 0; e < 4; ++e) {
            float v = rstd[i] * (acc[i][j][e] - mean[i] * c4[e]);
            __bf16 bv = (__bf16)v;
            vt[(4 * g + e) * (MB * 16) + i * 16 + r16] = __builtin_bit_cast(u16, bv);
          }
        }
        int d = lane >> 2, c = lane & 3;
        u32x4 v = *(const u32x4*)(vt + d * 32 + c * 8);
        *(u32x4*)(o2 + ((size_t)head * 16 + d) * S_ + tokb + c * 8) = v;
      }
    }
  }
}

constexpr int AT_KB = 384 * 32;
constexpr int AT_VS = 784;
constexpr int AT_VB = 18 * AT_VS;
constexpr int AT_HB = AT_KB + AT_VB;
__global__ __launch_bounds__(512, 2) void attn_k(const u16* __restrict__ Q, const u16* __restrict__ K,
                                                 const u16* __restrict__ Vt, u16* __restrict__ O2,
                                                 const u16* __restrict__ pf0, const u16* __restrict__ pf1) {
  __shared__ __attribute__((aligned(16))) char smem[2 * AT_HB];
  const int tid = threadIdx.x, lane = tid & 63;
  const int wid = __builtin_amdgcn_readfirstlane(tid >> 6);
  const int hp = blockIdx.x >> 4, qb = blockIdx.x & 15;
  const int hl = wid >> 2;
  const int head = hp * 2 + hl;
  const int qt = qb * 4 + (wid & 3);
  const int q0 = qt * 32;
  const int kw0 = qb * 128 - 256;
  const int i = lane & 31, hh = lane >> 5;

  {
    const int t = tid & 255;
    const u16* Kh = K + (size_t)head * S_ * 16;
    const u16* Vh = Vt + (size_t)head * 16 * S_;
    char* kl = smem + hl * AT_HB;
    char* vl = kl + AT_KB;
    u32x4 kv[3], vv[3];
#pragma unroll
    for (int r = 0; r < 3; ++r) {
      int c = t + 256 * r;
      int key = kw0 + (c >> 1); key = key < 0 ? 0 : key;
      kv[r] = *(const u32x4*)(Kh + key * 16 + (c & 1) * 8);
      int d = c / 48, cc = c % 48;
      int k2 = kw0 + cc * 8; k2 = k2 < 0 ? 0 : k2;
      vv[r] = *(const u32x4*)(Vh + d * S_ + k2);
    }
    if (t < 96) {
      int d = t / 48, cc = t % 48;
      unsigned f = (d == 0) ? 0x3F803F80u : 0u;
      *(u32x4*)(vl + (16 + d) * AT_VS + cc * 16) = (u32x4){f, f, f, f};
    }
#pragma unroll
    for (int r = 0; r < 3; ++r) {
      int c = t + 256 * r;
      int row = c >> 1;
      *(u32x4*)(kl + row * 32 + (((c & 1) ^ ((row >> 3) & 1)) << 4)) = kv[r];
      int d = c / 48, cc = c % 48;
      *(u32x4*)(vl + d * AT_VS + cc * 16) = vv[r];
    }
  }
  u32x4 qf = *(const u32x4*)(Q + ((size_t)head * S_ + q0 + i) * 16 + hh * 8);

  const int t0 = (q0 >= 256) ? 0 : (8 - qt);
  const int pi = ((i >> 2) & 1) * 16 + ((i >> 4) & 1) * 8 + ((i >> 3) & 1) * 4 + (i & 3);
  const float NEG = -1e30f;
  f32x16 c0, c8, zero;
#pragma unroll
  for (int r = 0; r < 16; ++r) {
    int kap = 16 * hh + r;
    zero[r] = 0.f;
    c0[r] = (kap > i) ? 0.f : NEG;
    c8[r] = (kap <= i) ? 0.f : NEG;
  }
  __syncthreads();
  u32x4 pfv[5];
  {
    const u32x4* q0p = (const u32x4*)pf0;
    const int gi = blockIdx.x * 512 + tid;
#pragma unroll
    for (int r = 0; r < 4; ++r) pfv[r] = q0p[gi + r * 131072];
    pfv[4] = ((const u32x4*)pf1)[gi < 57344 ? gi : 0];
  }

  const int wrow = (wid & 3) * 32;
  const char* kr = smem + hl * AT_HB + (wrow + pi) * 32 + ((hh ^ ((pi >> 3) & 1)) << 4);
  const char* vr = smem + hl * AT_HB + AT_KB + (i < 17 ? i : 17) * AT_VS + (wrow + 16 * hh) * 2;

  u32x4 kf[9];
#pragma unroll
  for (int t = 0; t < 9; ++t) kf[t] = *(const u32x4*)(kr + t * 1024);

  float mx = NEG;
#pragma unroll
  for (int t = 0; t < 9; ++t) {
    if (t >= t0) {
      f32x16 s = mfma32(kf[t], qf, t == 0 ? c0 : (t == 8 ? c8 : zero));
#pragma unroll
      for (int r = 0; r < 16; r += 2) mx = fmaxf(fmaxf(mx, s[r]), s[r + 1]);
    }
  }
  {
    unsigned u = __float_as_uint(mx);
    auto r = __builtin_amdgcn_permlane32_swap(u, u, false, false);
    mx = fmaxf(__uint_as_float(r[0]), __uint_as_float(r[1]));
  }
  f32x16 cm;
#pragma unroll
  for (int r = 0; r < 16; ++r) { cm[r] = -mx; c0[r] -= mx; c8[r] -= mx; }

  f32x16 oacc = zero;
#pragma unroll
  for (int t = 0; t < 9; ++t) {
    if (t >= t0) {
      u32x4 v0 = *(const u32x4*)(vr + t * 64);
      u32x4 v1 = *(const u32x4*)(vr + t * 64 + 16);
      f32x16 s = mfma32(kf[t], qf, t == 0 ? c0 : (t == 8 ? c8 : cm));
      float p[16];
#pragma unroll
      for (int r = 0; r < 16; ++r) p[r] = __builtin_amdgcn_exp2f(s[r]);
      u32x4 p0 = {pack2(p[0], p[1]), pack2(p[2], p[3]), pack2(p[4], p[5]), pack2(p[6], p[7])};
      u32x4 p1 = {pack2(p[8], p[9]), pack2(p[10], p[11]), pack2(p[12], p[13]), pack2(p[14], p[15])};
      oacc = mfma32(v0, p0, oacc);
      oacc = mfma32(v1, p1, oacc);
    }
  }
  float l;
  {
    unsigned u = __float_as_uint(oacc[8]);
    auto r = __builtin_amdgcn_permlane32_swap(u, u, false, false);
    l = __uint_as_float(r[0]) + __uint_as_float(r[1]);
  }
  float inv = 1.f / l;
  __syncthreads();
  {
    char* ot = smem + ((wid & 3) * 32 + i) * 64 + hl * 32 + hh * 8;
    *(u32x2*)(ot) = (u32x2){pack2(oacc[0] * inv, oacc[1] * inv), pack2(oacc[2] * inv, oacc[3] * inv)};
    *(u32x2*)(ot + 16) = (u32x2){pack2(oacc[4] * inv, oacc[5] * inv), pack2(oacc[6] * inv, oacc[7] * inv)};
  }
  __syncthreads();
  *(u32x4*)(O2 + ((size_t)hp * S_ + qb * 128) * 32 + tid * 8) = *(const u32x4*)(smem + tid * 16);
  asm volatile("" ::"v"(pfv[0]), "v"(pfv[1]), "v"(pfv[2]), "v"(pfv[3]), "v"(pfv[4]));
}

extern "C" void kernel_launch(void* const* d_in, const int* in_sizes, int n_in, void* d_out, int out_size,
                              void* d_ws, size_t ws_size, hipStream_t stream) {
  const u16* x = (const u16*)d_in[0];
  const u16* wqkv = (const u16*)d_in[1];
  const u16* wo = (const u16*)d_in[2];
  const u16* w_in = (const u16*)d_in[4];
  const u16* w_sp = (const u16*)d_in[5];
  const u16* b_sp = (const u16*)d_in[6];
  const u16* w_out = (const u16*)d_in[7];
  u16* out = (u16*)d_out;

  char* ws = (char*)d_ws;
  u16* Qb = (u16*)(ws);
  u16* Kb = (u16*)(ws + (2u << 20));
  u16* Vb = (u16*)(ws + (4u << 20));
  u16* Ob = (u16*)(ws + (6u << 20));
  u16* Hb = (u16*)(ws + (8u << 20));
  u16* Zb = (u16*)(ws + (10u << 20));
  u16* Ub = (u16*)(ws + (18u << 20));
  float* Pb = (float*)(ws + (22u << 20));

  gemm_k<128, 128, 4, 2, 1, 1, 8, 4, M_QKV, 512, 1536, 4, 6><<<256, 768, 0, stream>>>(
      x, wqkv, wo, w_in, nullptr, Qb, Kb, Vb, nullptr);
  attn_k<<<256, 512, 0, stream>>>(Qb, Kb, Vb, Ob, w_sp, w_out);
  gemm_k<64, 64, 2, 2, 2, 2, 4, 4, M_WO, 512, 512, 4, 8><<<256, 768, 0, stream>>>(
      Ob, wo, x, nullptr, nullptr, Hb, nullptr, nullptr, nullptr);
  gemm_k<128, 128, 4, 2, 1, 1, 8, 4, M_WIN, 512, 1792, 4, 7><<<224, 768, 0, stream>>>(
      Hb, w_in, nullptr, nullptr, nullptr, Zb, nullptr, nullptr, Pb);
  gemm_k<64, 128, 2, 1, 4, 2, 16, 3, M_SP, 2048, 1792, 4, 7><<<224, 768, 0, stream>>>(
      w_sp, Zb + FH_, Zb, b_sp, Pb, Ub, nullptr, nullptr, nullptr);
  gemm_k<64, 64, 2, 2, 2, 2, 7, 4, M_OUT, 896, 512, 4, 8><<<256, 768, 0, stream>>>(
      Ub, w_out, Hb, nullptr, nullptr, out, nullptr, nullptr, nullptr);
}
```

```cpp
#include <hip/hip_runtime.h>
#include <hip/hip_bf16.h>
#include <type_traits>

typedef unsigned short u16;
typedef short s16x8 __attribute__((ext_vector_type(8)));
typedef short s16x4 __attribute__((ext_vector_type(4)));
typedef float f32x4 __attribute__((ext_vector_type(4)));
typedef float f32x2 __attribute__((ext_vector_type(2)));
typedef float f32x16 __attribute__((ext_vector_type(16)));
typedef __bf16 bf16x2 __attribute__((ext_vector_type(2)));
typedef __bf16 bf16x8 __attribute__((ext_vector_type(8)));
typedef unsigned u32x4 __attribute__((ext_vector_type(4)));
typedef unsigned u32x2 __attribute__((ext_vector_type(2)));

#define DI __device__ __forceinline__
#define SCHED_FENCE __builtin_amdgcn_sched_barrier(0)

constexpr int S_ = 2048, D_ = 512, H_ = 32, DH_ = 16, F_ = 1792, FH_ = 896;

DI unsigned pack2(float a, float b) {
  bf16x2 v = {(__bf16)a, (__bf16)b};
  return __builtin_bit_cast(unsigned, v);
}
DI float bflo(unsigned u) { return __uint_as_float(u << 16); }
DI float bfhi(unsigned u) { return __uint_as_float(u & 0xffff0000u); }
DI float rbf(float a) {
  __bf16 v = (__bf16)a;
  return __uint_as_float(((unsigned)__builtin_bit_cast(u16, v)) << 16);
}
DI float red4(float v) {
  unsigned u = __float_as_uint(v);
  auto r = __builtin_amdgcn_permlane16_swap(u, u, false, false);
  float a = __uint_as_float(r[0]) + __uint_as_float(r[1]);
  unsigned w = __float_as_uint(a);
  auto q = __builtin_amdgcn_permlane32_swap(w, w, false, false);
  return __uint_as_float(q[0]) + __uint_as_float(q[1]);
}
DI float rowsum16(float v) {
  v += __int_as_float(__builtin_amdgcn_update_dpp(0, __float_as_int(v), 0x111, 0xf, 0xf, true));
  v += __int_as_float(__builtin_amdgcn_update_dpp(0, __float_as_int(v), 0x112, 0xf, 0xf, true));
  v += __int_as_float(__builtin_amdgcn_update_dpp(0, __float_as_int(v), 0x114, 0xf, 0xf, true));
  v += __int_as_float(__builtin_amdgcn_update_dpp(0, __float_as_int(v), 0x118, 0xf, 0xf, true));
  return v;
}
DI float dot2(unsigned a, unsigned b, float c) {
  return __builtin_amdgcn_fdot2_f32_bf16(__builtin_bit_cast(bf16x2, a), __builtin_bit_cast(bf16x2, b), c, false);
}
DI f32x4 mfma16(u32x4 a, u32x4 b, f32x4 c) {
  return __builtin_amdgcn_mfma_f32_16x16x32_bf16(__builtin_bit_cast(bf16x8, a), __builtin_bit_cast(bf16x8, b), c, 0, 0, 0);
}
DI f32x16 mfma32(u32x4 a, u32x4 b, f32x16 c) {
  return __builtin_amdgcn_mfma_f32_32x32x16_bf16(__builtin_bit_cast(bf16x8, a), __builtin_bit_cast(bf16x8, b), c, 0, 0, 0);
}
template <int N> DI void wait_vm() { asm volatile("s_waitcnt vmcnt(%0)" ::"n"(N) : "memory"); }
DI void wait_lgkm0() { asm volatile("s_waitcnt lgkmcnt(0)" ::: "memory"); }
DI void bar() {
  asm volatile("" ::: "memory");
  __builtin_amdgcn_s_barrier();
  asm volatile("" ::: "memory");
}

template <int OFF> DI u32x2 tr_read(unsigned addr) {
  u32x2 r;
  asm volatile("ds_read_b64_tr_b16 %0, %1 offset:%2" : "=v"(r) : "v"(addr), "n"(OFF));
  return r;
}
template <int OFF> DI u32x4 lds_read128(unsigned addr) {
  u32x4 r;
  asm volatile("ds_read_b128 %0, %1 offset:%2" : "=v"(r) : "v"(addr), "n"(OFF));
  return r;
}
template <int N> DI void wait_lgkm() { asm volatile("s_waitcnt lgkmcnt(%0)" ::"n"(N) : "memory"); }
template <int I, int N, class F> DI void static_for(F&& f) {
  if constexpr (I < N) {
    f(std::integral_constant<int, I>{});
    static_for<I + 1, N>(f);
  }
}
DI void glds16(const void* g, void* l) {
  __builtin_amdgcn_global_load_lds((const unsigned*)g, (unsigned*)l, 16, 0, 0);
}

template <int KT, int NS> constexpr int iss_before(int kt) {
  int is = 2;
  for (int k = 0; k < kt; ++k) {
    int n = is + 2;
    if (n > k + NS) n = k + NS;
    if (n > KT) n = KT;
    is = n;
  }
  return is;
}
enum { M_QKV = 0, M_WO = 1, M_WIN = 2, M_SP = 3, M_OUT = 4 };

template <int BM, int BN, int WM, int WN, int WK, int SUB, int KT, int NS, int MODE, int LDA, int LDB, int PM, int PN>
__global__ __launch_bounds__(768, 3) void gemm_k(const u16* __restrict__ A, const u16* __restrict__ B,
                                                 const u16* __restrict__ e0, const u16* __restrict__ e1,
                                                 const float* __restrict__ partin, u16* __restrict__ o0,
                                                 u16* __restrict__ o1, u16* __restrict__ o2,
                                                 float* __restrict__ partout) {
  static_assert(WM * WN * WK == 8, "8 consumer waves");
  constexpr int AB = BM * 128, BB = BN * 128, SUBB = AB + BB, STG = SUB * SUBB;
  constexpr int AI = AB / 1024, BI = BB / 1024;
  static_assert(AI % 4 == 0, "A instr");
  constexpr int LA = AI / 4;
  constexpr int LBF = BI / 4, LBR = BI % 4;
  constexpr int MB = BM / WM / 16, NB = BN / WN / 16;
  constexpr int RING = NS * STG;
  constexpr int EXTRA = (MODE == M_SP) ? 16384 : 0;
  constexpr bool LNA = (MODE == M_QKV || MODE == M_WIN);
  __shared__ __attribute__((aligned(16))) char smem[RING + EXTRA];

  const int tid = threadIdx.x, lane = tid & 63;
  const int wid = __builtin_amdgcn_readfirstlane(tid >> 6);

  if constexpr (MODE == M_QKV) {
    if (blockIdx.x >= 192) {
      const int pb = blockIdx.x - 192;
      u32x4 accx = {0u, 0u, 0u, 0u};
      auto pf = [&](const u16* p, int n16) {
        const u32x4* q = (const u32x4*)p;
        for (int i = pb * 768 + tid; i < n16; i += 64 * 768 * 4) {
          u32x4 a = q[i];
          u32x4 b = (i + 64 * 768 < n16) ? q[i + 64 * 768] : a;
          u32x4 c = (i + 2 * 64 * 768 < n16) ? q[i + 2 * 64 * 768] : a;
          u32x4 d = (i + 3 * 64 * 768 < n16) ? q[i + 3 * 64 * 768] : a;
          accx ^= a ^ b ^ c ^ d;
        }
      };
      pf(e0, 512 * 512 / 8);
      pf(e1, 512 * 1792 / 8);
      asm volatile("" ::"v"(accx));
      return;
    }
  }
  constexpr int TN = (MODE == M_QKV ? 1536 : MODE == M_WIN ? 1792 : MODE == M_SP ? 896 : 512) / BN;
  constexpr int PXN = TN / PN;
  const int bid = blockIdx.x, xcd = bid & 7, jj = bid >> 3;
  const int tm = (xcd / PXN) * PM + jj / PN, tn = (xcd % PXN) * PN + jj % PN;
  const int m0 = tm * BM, n0 = tn * BN;

  if (wid >= 8) {
    const int lw = wid - 8;
    unsigned aoff[LA], boff[LBF + 1];
#pragma unroll
    for (int i = 0; i < LA; ++i) {
      int q = lw + 4 * i;
      int row = q * 8 + (lane >> 3), pos = lane & 7;
      int ch = pos ^ (row & 7);
      if constexpr (MODE == M_WO) aoff[i] = (unsigned)(((ch >> 2) * S_ + m0 + row) * 32 + (ch & 3) * 8);
      else aoff[i] = (unsigned)((m0 + row) * LDA + (ch << 3));
    }
#pragma unroll
    for (int i = 0; i < LBF; ++i) {
      int q = lw + 4 * i;
      int r = (q & 7) * 8 + (lane >> 3), pos = lane & 7;
      int h = ((r >> 1) & 1) | (((r >> 3) & 1) << 1);
      int ch = (((pos >> 1) ^ h) << 1) | (pos & 1);
      boff[i] = (unsigned)(r * LDB + n0 + (q >> 3) * 64 + ch * 8);
    }
    static_assert(LBR == 0, "BN % 32");
    const bool extraB = (lw < LBR);
    auto issue = [&](int kt, int slot) {
#pragma unroll
      for (int sb = 0; sb < SUB; ++sb) {
        const u16* a = A + (kt * SUB + sb) * (MODE == M_WO ? 2 * S_ * 32 : 64);
        const u16* b = B + (size_t)(kt * SUB + sb) * 64 * LDB;
        char* s = smem + slot * STG + sb * SUBB;
#pragma unroll
        for (int i = 0; i < LA; ++i) glds16(a + aoff[i], s + (lw + 4 * i) * 1024);
#pragma unroll
        for (int i = 0; i < LBF; ++i) glds16(b + boff[i], s + AB + (lw + 4 * i) * 1024);
        if (LBR != 0) {
          if (extraB) glds16(b + boff[LBF], s + AB + (lw + 4 * LBF) * 1024);
        }
      }
    };
    issue(0, 0);
    issue(1, 1);
    static_for<0, KT>([&](auto KTI) {
      constexpr int kt = KTI;
      constexpr int is0 = iss_before<KT, NS>(kt);
      constexpr int is1 = iss_before<KT, NS>(kt + 1);
      constexpr int later = is0 - 1 - kt;
      if (LBR != 0 && extraB) wait_vm<later * SUB * (LA + LBF + 1)>();
      else wait_vm<later * SUB * (LA + LBF)>();
      bar();
      if constexpr (is1 > is0) issue(is0, is0 % NS);
      if constexpr (is1 > is0 + 1) issue(is0 + 1, (is0 + 1) % NS);
    });
    return;
  }

  const int wk = wid % WK;
  const int wmn = wid / WK;
  const int wm = wmn / WN, wn = wmn % WN;
  const int r16 = lane & 15, g = lane >> 4;
  const int tokb = m0 + wm * (BM / WM);
  const int nbase = n0 + wn * (BN / WN);

  if constexpr (MODE == M_SP) {
    float* sr = (float*)(smem + RING);
    float* srm = sr + 2048;
#pragma unroll
    for (int j = 0; j < 2; ++j) {
      int row = 2 * tid + 1024 * j;
      f32x4 t = {0.f, 0.f, 0.f, 0.f};
#pragma unroll
      for (int p = 0; p < 7; ++p) {
        f32x4 v = *(const f32x4*)(partin + (size_t)(p * 2048 + row) * 2);
        t += v;
      }
      float mean0 = t[0] * (1.f / 896.f), mean1 = t[2] * (1.f / 896.f);
      float var0 = fmaxf(t[1] * (1.f / 896.f) - mean0 * mean0, 0.f);
      float var1 = fmaxf(t[3] * (1.f / 896.f) - mean1 * mean1, 0.f);
      float r0 = rsqrtf(var0 + 1e-5f), r1 = rsqrtf(var1 + 1e-5f);
      *(f32x2*)(sr + row) = (f32x2){r0, r1};
      *(f32x2*)(srm + row) = (f32x2){r0 * mean0, r1 * mean1};
    }
  }

  f32x4 acc[MB][NB];
#pragma unroll
  for (int i = 0; i < MB; ++i)
#pragma unroll
    for (int j = 0; j < NB; ++j) acc[i][j] = (f32x4){0.f, 0.f, 0.f, 0.f};
  float s1[MB], s2[MB], cs[NB];
  f32x2 cc2[MB];
#pragma unroll
  for (int i = 0; i < MB; ++i) { s1[i] = 0.f; s2[i] = 0.f; cc2[i] = (f32x2){0.f, 0.f}; }
#pragma unroll
  for (int j = 0; j < NB; ++j) cs[j] = 0.f;

  constexpr int NKS = 2 * SUB / WK;
  static_assert(NKS >= 1 && NKS <= 2, "NKS");
  static_assert(NKS == 1 || WK <= 2, "NKS");
  const unsigned lbase = (unsigned)(size_t)smem;
  unsigned ard[NKS], bpo[4];
  {
    int q = (lane & 15) >> 2, p = lane & 3;
    int row = wm * (BM / WM) + r16;
#pragma unroll
    for (int kq = 0; kq < NKS; ++kq) {
      int kk = wk + WK * kq;
      int sub = kk >> 1, ks = kk & 1;
      ard[kq] = lbase + sub * SUBB + row * 128 + (((4 * ks + g) ^ (row & 7)) << 4);
    }
    int h = ((q >> 1) & 1) | ((g & 1) << 1);
    int kk0 = wk;
    unsigned bb = lbase + (kk0 >> 1) * SUBB + AB + ((kk0 & 1) * 32 + 8 * g + q) * 128 + p * 8;
    constexpr int NBW = BN / WN / 16;
    static_assert(NBW % 4 == 0 || NBW == 2, "wave n-blocks");
    int nb0 = wn * NBW;
#pragma unroll
    for (int pc = 0; pc < 4; ++pc) {
      int nb = nb0 + pc;
      bpo[pc] = bb + (nb >> 2) * 8192 + (((nb & 3) ^ h) << 5);
    }
  }
  unsigned strd = lbase + RING + ((wk & 1) * 32 + (wk >> 1) * 64 + g * 8) * 4;

  constexpr int ST = (MODE == M_SP) ? 4 : 0;
  constexpr int PER = ST + MB + 2 * NB;
  constexpr int RTOT = NKS * PER;
  constexpr int NB0 = (NB + 1) / 2;

  int slot = 0;
#pragma unroll 1
  for (int kt = 0; kt < KT; ++kt) {
    wait_lgkm0();
    bar();
    const unsigned so = slot * STG;
    u32x4 af[NKS][MB];
    u32x2 bl[NKS][NB], bh[NKS][NB];
    u32x4 st[4];
    static_for<0, NKS>([&](auto KQ) {
      if constexpr (MODE == M_SP) {
        st[0] = lds_read128<0>(strd);
        st[1] = lds_read128<16>(strd);
        st[2] = lds_read128<8192>(strd);
        st[3] = lds_read128<8208>(strd);
      }
      static_for<0, MB>([&](auto I) { af[KQ][I] = lds_read128<I * 2048>(ard[KQ] + so); });
      static_for<0, NB>([&](auto J) {
        constexpr int off = KQ * (WK == 1 ? 4096 : SUBB) + (J >> 2) * 8192;
        bl[KQ][J] = tr_read<off>(bpo[J & 3] + so);
        bh[KQ][J] = tr_read<off + 512>(bpo[J & 3] + so);
      });
    });
    if constexpr (MODE == M_SP) strd += SUB * 256;
    const bool docs = LNA && ((kt % WM) == wm);
    static_for<0, NKS>([&](auto KQ) {
      constexpr int w0 = RTOT - (KQ * PER + ST + MB + 2 * NB0);
      constexpr int w1 = RTOT - (KQ + 1) * PER;
      constexpr int wa = RTOT - (KQ * PER + ST + MB);
      wait_lgkm<(wa > 15 ? 15 : wa)>();
      u32x4 a[MB];
#pragma unroll
      for (int i = 0; i < MB; ++i) {
        asm volatile("" : "+v"(af[KQ][i]));
        a[i] = af[KQ][i];
      }
      if constexpr (MODE == M_SP) {
        asm volatile("" : "+v"(st[0]), "+v"(st[1]), "+v"(st[2]), "+v"(st[3]));
        f32x4 r0 = __builtin_bit_cast(f32x4, st[0]), r1 = __builtin_bit_cast(f32x4, st[1]);
        f32x4 m0v = __builtin_bit_cast(f32x4, st[2]), m1v = __builtin_bit_cast(f32x4, st[3]);
#pragma unroll
        for (int i = 0; i < MB; ++i) {
          f32x2 w01 = {bflo(a[i][0]), bfhi(a[i][0])}, w23 = {bflo(a[i][1]), bfhi(a[i][1])};
          f32x2 w45 = {bflo(a[i][2]), bfhi(a[i][2])}, w67 = {bflo(a[i][3]), bfhi(a[i][3])};
          f32x2 c = cc2[i];
          c = __builtin_elementwise_fma(w01, (f32x2){m0v[0], m0v[1]}, c);
          c = __builtin_elementwise_fma(w23, (f32x2){m0v[2], m0v[3]}, c);
          c = __builtin_elementwise_fma(w45, (f32x2){m1v[0], m1v[1]}, c);
          c = __builtin_elementwise_fma(w67, (f32x2){m1v[2], m1v[3]}, c);
          cc2[i] = c;
          w01 *= (f32x2){r0[0], r0[1]}; w23 *= (f32x2){r0[2], r0[3]};
          w45 *= (f32x2){r1[0], r1[1]}; w67 *= (f32x2){r1[2], r1[3]};
          a[i][0] = pack2(w01[0], w01[1]);
          a[i][1] = pack2(w23[0], w23[1]);
          a[i][2] = pack2(w45[0], w45[1]);
          a[i][3] = pack2(w67[0], w67[1]);
        }
      }
      if constexpr (LNA) {
#pragma unroll
        for (int i = 0; i < MB; ++i) {
          if constexpr (WN == 2) {
#pragma unroll
            for (int e = 0; e < 4; ++e) s1[i] = dot2(a[i][e], wn ? a[i][e] : 0x3F803F80u, s1[i]);
          } else {
#pragma unroll
            for (int e = 0; e < 4; ++e) {
              s1[i] = dot2(a[i][e], 0x3F803F80u, s1[i]);
              s2[i] = dot2(a[i][e], a[i][e], s2[i]);
            }
          }
        }
      }
#pragma unroll
      for (int i = 0; i < MB; ++i) asm volatile("" : "+v"(a[i]));
      wait_lgkm<(w0 > 15 ? 15 : w0)>();
#pragma unroll
      for (int j = 0; j < NB0; ++j) {
        asm volatile("" : "+v"(bl[KQ][j]), "+v"(bh[KQ][j]));
        u32x4 b = {bl[KQ][j][0], bl[KQ][j][1], bh[KQ][j][0], bh[KQ][j][1]};
#pragma unroll
        for (int i = 0; i < MB; ++i) acc[i][j] = mfma16(b, a[i], acc[i][j]);
      }
      wait_lgkm<(w1 > 15 ? 15 : w1)>();
#pragma unroll
      for (int j = NB0; j < NB; ++j) {
        asm volatile("" : "+v"(bl[KQ][j]), "+v"(bh[KQ][j]));
        u32x4 b = {bl[KQ][j][0], bl[KQ][j][1], bh[KQ][j][0], bh[KQ][j][1]};
#pragma unroll
        for (int i = 0; i < MB; ++i) acc[i][j] = mfma16(b, a[i], acc[i][j]);
      }
      if constexpr (LNA) {
        if (docs) {
#pragma unroll
          for (int j = 0; j < NB; ++j) {
            cs[j] = dot2(bl[KQ][j][0], 0x3F803F80u, cs[j]);
            cs[j] = dot2(bl[KQ][j][1], 0x3F803F80u, cs[j]);
            cs[j] = dot2(bh[KQ][j][0], 0x3F803F80u, cs[j]);
            cs[j] = dot2(bh[KQ][j][1], 0x3F803F80u, cs[j]);
          }
        }
      }
    });
    ++slot; if (slot == NS) slot = 0;
  }

  if constexpr (MODE != M_QKV) {
    constexpr int RSF = BN * 4 + 16;
    constexpr int PLB = BM * RSF;
    constexpr int O_ROWP = WK * PLB;
    constexpr int O_COLP = O_ROWP + WK * BM * 8;
    constexpr int O_ROWF = O_COLP + 8 * BN * 4;
    constexpr int O_COLF = O_ROWF + BM * 8;
    static_assert(O_COLF + BN * 4 <= RING, "epilogue LDS");
    constexpr int CH = BN / 8;
    constexpr int LPR = (CH <= 8) ? 8 : 16;
    constexpr int RPP = 512 / LPR;
    constexpr int NPASS = BM / RPP;
    const int ec = tid & (LPR - 1), er = tid / LPR;
    const bool eact = ec < CH;
    u32x4 eop[NPASS];
    if constexpr (MODE != M_WIN) {
      constexpr int LDE = (MODE == M_SP) ? F_ : D_;
#pragma unroll
      for (int ps = 0; ps < NPASS; ++ps)
        if (eact) eop[ps] = *(const u32x4*)(e0 + (size_t)(m0 + er + ps * RPP) * LDE + n0 + ec * 8);
    }
    wait_lgkm0();
    bar();
    {
      char* pl = smem + wk * PLB + (wm * (BM / WM) + r16) * RSF + (wn * (BN / WN) + 4 * g) * 4;
#pragma unroll
      for (int i = 0; i < MB; ++i)
#pragma unroll
        for (int j = 0; j < NB; ++j) *(f32x4*)(pl + i * 16 * RSF + j * 64) = acc[i][j];
    }
    if constexpr (MODE == M_WIN || MODE == M_SP) {
#pragma unroll
      for (int i = 0; i < MB; ++i) {
        if constexpr (MODE == M_WIN && WN == 2) {
          float a = red4(s1[i]);
          if (g == 0) *(float*)(smem + O_ROWP + (wk * BM + wm * (BM / WM) + i * 16 + r16) * 8 + wn * 4) = a;
        } else {
          float a = (MODE == M_WIN) ? red4(s1[i]) : red4(cc2[i][0] + cc2[i][1]);
          float b = (MODE == M_WIN) ? red4(s2[i]) : 0.f;
          if (g == 0) *(f32x2*)(smem + O_ROWP + (wk * BM + wm * (BM / WM) + i * 16 + r16) * 8) = (f32x2){a, b};
        }
      }
    }
    if constexpr (MODE == M_WIN) {
#pragma unroll
      for (int j = 0; j < NB; ++j) {
        float v = red4(cs[j]);
        if (g == 0) *(float*)(smem + O_COLP + ((wm * WK + wk) * BN + wn * (BN / WN) + j * 16 + r16) * 4) = v;
      }
    }
    wait_lgkm0();
    bar();
    if constexpr (MODE == M_WIN) {
      if (tid < BN) {
        float c = 0.f;
#pragma unroll
        for (int w = 0; w < WM * WK; ++w) c += *(const float*)(smem + O_COLP + (w * BN + tid) * 4);
        *(float*)(smem + O_COLF + tid * 4) = c;
      } else if (tid >= 128 && tid < 128 + BM) {
        int row = tid - 128;
        f32x2 t = {0.f, 0.f};
#pragma unroll
        for (int k = 0; k < WK; ++k) t += *(const f32x2*)(smem + O_ROWP + (k * BM + row) * 8);
        float mean = t[0] * (1.f / 512.f);
        float var = fmaxf(t[1] * (1.f / 512.f) - mean * mean, 0.f);
        *(f32x2*)(smem + O_ROWF + row * 8) = (f32x2){mean, rsqrtf(var + 1e-5f)};
      }
      wait_lgkm0();
      bar();
    }
    f32x4 cf0 = {0.f, 0.f, 0.f, 0.f}, cf1 = cf0;
    if constexpr (MODE == M_WIN) {
      if (eact) {
        cf0 = *(const f32x4*)(smem + O_COLF + ec * 32);
        cf1 = *(const f32x4*)(smem + O_COLF + ec * 32 + 16);
      }
    }
#pragma unroll
    for (int ps = 0; ps < NPASS; ++ps) {
      const int row = er + ps * RPP;
      const int tok = m0 + row;
      float st1 = 0.f, st2 = 0.f;
      if (eact) {
        f32x4 v0 = *(const f32x4*)(smem + row * RSF + ec * 32);
        f32x4 v1 = *(const f32x4*)(smem + row * RSF + ec * 32 + 16);
#pragma unroll
        for (int k = 1; k < WK; ++k) {
          v0 += *(const f32x4*)(smem + k * PLB + row * RSF + ec * 32);
          v1 += *(const f32x4*)(smem + k * PLB + row * RSF + ec * 32 + 16);
        }
        float v[8] = {v0[0], v0[1], v0[2], v0[3], v1[0], v1[1], v1[2], v1[3]};
        u32x4 ov;
        if constexpr (MODE == M_WO || MODE == M_OUT) {
#pragma unroll
          for (int e = 0; e < 4; ++e)
            ov[e] = pack2(bflo(eop[ps][e]) + rbf(v[2 * e]), bfhi(eop[ps][e]) + rbf(v[2 * e + 1]));
          *(u32x4*)(o0 + (size_t)tok * D_ + n0 + ec * 8) = ov;
        } else if constexpr (MODE == M_SP) {
          float c = 0.f;
#pragma unroll
          for (int k = 0; k < WK; ++k) c += *(const float*)(smem + O_ROWP + (k * BM + row) * 8);
          const float bias = bflo((unsigned)e1[tok]);
#pragma unroll
          for (int e = 0; e < 4; ++e) {
            float g0 = rbf(rbf(v[2 * e] - c) + bias), g1 = rbf(rbf(v[2 * e + 1] - c) + bias);
            ov[e] = pack2(bflo(eop[ps][e]) * g0, bfhi(eop[ps][e]) * g1);
          }
          *(u32x4*)(o0 + (size_t)tok * FH_ + n0 + ec * 8) = ov;
        } else {
          f32x2 ms = *(const f32x2*)(smem + O_ROWF + row * 8);
          float cf[8] = {cf0[0], cf0[1], cf0[2], cf0[3], cf1[0], cf1[1], cf1[2], cf1[3]};
          float zz[8];
#pragma unroll
          for (int e = 0; e < 8; ++e) {
            float x = rbf(ms[1] * (v[e] - ms[0] * cf[e]));
            float a = x * fmaf(x * x, -0.10294324f, -2.3022082f);
            float t = __builtin_amdgcn_exp2f(a);
            zz[e] = x * __builtin_amdgcn_rcpf(1.f + t);
          }
#pragma unroll
          for (int e = 0; e < 4; ++e) ov[e] = pack2(zz[2 * e], zz[2 * e + 1]);
          *(u32x4*)(o0 + (size_t)tok * F_ + n0 + ec * 8) = ov;
          if (n0 >= FH_) {
#pragma unroll
            for (int e = 0; e < 4; ++e) {
              st1 = dot2(ov[e], 0x3F803F80u, st1);
              st2 = dot2(ov[e], ov[e], st2);
            }
          }
        }
      }
      if constexpr (MODE == M_WIN) {
        if (n0 >= FH_) {
          static_assert(LPR == 16, "row reduce");
          st1 = rowsum16(st1);
          st2 = rowsum16(st2);
          if (ec == 15) *(f32x2*)(partout + (size_t)(((n0 - FH_) / BN) * 2048 + tok) * 2) = (f32x2){st1, st2};
        }
      }
    }
    return;
  }
  if constexpr (MODE == M_QKV) {
    static_assert(WK == 1 && WN == 2 && MB == 2, "qkv epilogue");
    wait_lgkm0();
    bar();
    float* csl = (float*)smem;
    f32x4* ex = (f32x4*)(smem + 4096);
#pragma unroll
    for (int j = 0; j < NB; ++j) {
      float v = red4(cs[j]);
      if (g == 0) csl[wid * (NB * 16) + j * 16 + r16] = v;
    }
    ex[wid * 64 + lane] = (f32x4){s1[0], 0.f, s1[1], 0.f};
    wait_lgkm0();
    bar();
    {
      f32x4 o = ex[(wid ^ 1) * 64 + lane];
      float m0_ = wn ? o[0] : s1[0], q0_ = wn ? s1[0] : o[0];
      float m1_ = wn ? o[2] : s1[1], q1_ = wn ? s1[1] : o[2];
      s1[0] = m0_; s2[0] = q0_;
      s1[1] = m1_; s2[1] = q1_;
    }
    float mean[MB], rstd[MB];
#pragma unroll
    for (int i = 0; i < MB; ++i) {
      float a = red4(s1[i]), b = red4(s2[i]);
      mean[i] = a * (1.f / 512.f);
      float var = fmaxf(b * (1.f / 512.f) - mean[i] * mean[i], 0.f);
      rstd[i] = rsqrtf(var + 1e-5f);
    }
    const int which = n0 >> 9;
#pragma unroll
    for (int j = 0; j < NB; ++j) {
      f32x4 c4 = {0.f, 0.f, 0.f, 0.f};
#pragma unroll
      for (int w = 0; w < 4; ++w) c4 += *(const f32x4*)(csl + (w * 2 + wn) * (NB * 16) + j * 16 + 4 * g);
      const int n = nbase + j * 16;
      const int head = (n & 511) >> 4;
      if (which < 2) {
        u16* dst = (which == 0 ? o0 : o1);
        const float sc = (which == 0) ? 0.36067376022224085f : 1.f;
#pragma unroll
        for (int i = 0; i < MB; ++i) {
          int tok = tokb + i * 16 + r16;
          float rs = rstd[i] * sc;
          float v0 = rs * (acc[i][j][0] - mean[i] * c4[0]);
          float v1 = rs * (acc[i][j][1] - mean[i] * c4[1]);
          float v2 = rs * (acc[i][j][2] - mean[i] * c4[2]);
          float v3 = rs * (acc[i][j][3] - mean[i] * c4[3]);
          *(u32x2*)(dst + ((size_t)head * S_ + tok) * 16 + 4 * g) = (u32x2){pack2(v0, v1), pack2(v2, v3)};
        }
      } else {
        u16* vt = (u16*)(smem + 16384 + (wid * NB + j) * (MB * 512));
#pragma unroll
        for (int i = 0; i < MB; ++i) {
#pragma unroll
          for (int e = 0; e < 4; ++e) {
            float v = rstd[i] * (acc[i][j][e] - mean[i] * c4[e]);
            __bf16 bv = (__bf16)v;
            vt[(4 * g + e) * (MB * 16) + i * 16 + r16] = __builtin_bit_cast(u16, bv);
          }
        }
        int d = lane >> 2, c = lane & 3;
        u32x4 v = *(const u32x4*)(vt + d * 32 + c * 8);
        *(u32x4*)(o2 + ((size_t)head * 16 + d) * S_ + tokb + c * 8) = v;
      }
    }
  }
}

constexpr int AT_KB = 384 * 32;
constexpr int AT_VS = 784;
constexpr int AT_VB = 18 * AT_VS;
constexpr int AT_QB = 128 * 32;
constexpr int AT_HB = AT_KB + AT_VB + AT_QB;
__global__ __launch_bounds__(512, 2) void attn_k(const u16* __restrict__ Q, const u16* __restrict__ K,
                                                 const u16* __restrict__ Vt, u16* __restrict__ O2,
                                                 const u16* __restrict__ pf0, const u16* __restrict__ pf1) {
  __shared__ __attribute__((aligned(16))) char smem[2 * AT_HB];
  const int tid = threadIdx.x, lane = tid & 63;
  const int wid = __builtin_amdgcn_readfirstlane(tid >> 6);
  const int hp = blockIdx.x >> 4, qb = blockIdx.x & 15;
  const int hl = wid >> 2;
  const int head = hp * 2 + hl;
  const int qt = qb * 4 + (wid & 3);
  const int q0 = qt * 32;
  const int kw0 = qb * 128 - 256;
  const int i = lane & 31, hh = lane >> 5;

  {
    const int w4 = wid & 3;
    const u16* Qh = Q + ((size_t)head * S_ + qb * 128) * 16;
    const u16* Kh = K + (size_t)head * S_ * 16;
    const u16* Vh = Vt + (size_t)head * 16 * S_;
    char* kl = smem + hl * AT_HB;
    char* vl = kl + AT_KB;
    char* ql = vl + AT_VB;
    glds16(Qh + (w4 * 64 + lane) * 8, ql + w4 * 1024);
#pragma unroll
    for (int r = 0; r < 3; ++r) {
      int c = (w4 * 3 + r) * 64 + lane;
      int row = c >> 1;
      int key = kw0 + row; key = key < 0 ? 0 : key;
      glds16(Kh + key * 16 + (((c & 1) ^ ((row >> 3) & 1)) << 3), kl + (w4 * 3 + r) * 1024);
    }
    {
      int k2 = kw0 + lane * 8; k2 = k2 < 0 ? 0 : k2;
      if (lane < 48) {
#pragma unroll
        for (int r = 0; r < 4; ++r) glds16(Vh + (w4 * 4 + r) * S_ + k2, vl + (w4 * 4 + r) * AT_VS);
      }
    }
    const int t = tid & 255;
    if (t < 96) {
      int d = t / 48, cc = t % 48;
      unsigned f = (d == 0) ? 0x3F803F80u : 0u;
      *(u32x4*)(vl + (16 + d) * AT_VS + cc * 16) = (u32x4){f, f, f, f};
    }
  }

  const int t0 = (q0 >= 256) ? 0 : (8 - qt);
  const int pi = ((i >> 2) & 1) * 16 + ((i >> 4) & 1) * 8 + ((i >> 3) & 1) * 4 + (i & 3);
  const float NEG = -1e30f;
  f32x16 c0, c8, zero;
#pragma unroll
  for (int r = 0; r < 16; ++r) {
    int kap = 16 * hh + r;
    zero[r] = 0.f;
    c0[r] = (kap > i) ? 0.f : NEG;
    c8[r] = (kap <= i) ? 0.f : NEG;
  }
  wait_vm<4>();
  wait_lgkm0();
  bar();
  const int wrow = (wid & 3) * 32;
  const char* kr = smem + hl * AT_HB + (wrow + pi) * 32 + ((hh ^ ((pi >> 3) & 1)) << 4);
  const char* vr = smem + hl * AT_HB + AT_KB + (i < 17 ? i : 17) * AT_VS + (wrow + 16 * hh) * 2;

  const u32x4 qf = *(const u32x4*)(smem + hl * AT_HB + AT_KB + AT_VB + (wrow + i) * 32 + hh * 16);
  u32x4 kf[9];
#pragma unroll
  for (int t = 0; t < 9; ++t) kf[t] = *(const u32x4*)(kr + t * 1024);

  float mx = NEG;
#pragma unroll
  for (int t = 0; t < 9; ++t) {
    if (t >= t0) {
      f32x16 s = mfma32(kf[t], qf, t == 0 ? c0 : (t == 8 ? c8 : zero));
#pragma unroll
      for (int r = 0; r < 16; r += 2) mx = fmaxf(fmaxf(mx, s[r]), s[r + 1]);
    }
  }
  {
    unsigned u = __float_as_uint(mx);
    auto r = __builtin_amdgcn_permlane32_swap(u, u, false, false);
    mx = fmaxf(__uint_as_float(r[0]), __uint_as_float(r[1]));
  }
  f32x16 cm;
#pragma unroll
  for (int r = 0; r < 16; ++r) { cm[r] = -mx; c0[r] -= mx; c8[r] -= mx; }
  wait_vm<0>();
  bar();
  u32x4 pfv[5];
  {
    const u32x4* q0p = (const u32x4*)pf0;
    const int gi = blockIdx.x * 512 + tid;
#pragma unroll
    for (int r = 0; r < 4; ++r) pfv[r] = q0p[gi + r * 131072];
    pfv[4] = ((const u32x4*)pf1)[gi < 57344 ? gi : 0];
  }


  f32x16 oacc = zero;
#pragma unroll
  for (int t = 0; t < 9; ++t) {
    if (t >= t0) {
      u32x4 v0 = *(const u32x4*)(vr + t * 64);
      u32x4 v1 = *(const u32x4*)(vr + t * 64 + 16);
      f32x16 s = mfma32(kf[t], qf, t == 0 ? c0 : (t == 8 ? c8 : cm));
      float p[16];
#pragma unroll
      for (int r = 0; r < 16; ++r) p[r] = __builtin_amdgcn_exp2f(s[r]);
      u32x4 p0 = {pack2(p[0], p[1]), pack2(p[2], p[3]), pack2(p[4], p[5]), pack2(p[6], p[7])};
      u32x4 p1 = {pack2(p[8], p[9]), pack2(p[10], p[11]), pack2(p[12], p[13]), pack2(p[14], p[15])};
      oacc = mfma32(v0, p0, oacc);
      oacc = mfma32(v1, p1, oacc);
    }
  }
  float l;
  {
    unsigned u = __float_as_uint(oacc[8]);
    auto r = __builtin_amdgcn_permlane32_swap(u, u, false, false);
    l = __uint_as_float(r[0]) + __uint_as_float(r[1]);
  }
  float inv = 1.f / l;
  __syncthreads();
  {
    char* ot = smem + ((wid & 3) * 32 + i) * 64 + hl * 32 + hh * 8;
    *(u32x2*)(ot) = (u32x2){pack2(oacc[0] * inv, oacc[1] * inv), pack2(oacc[2] * inv, oacc[3] * inv)};
    *(u32x2*)(ot + 16) = (u32x2){pack2(oacc[4] * inv, oacc[5] * inv), pack2(oacc[6] * inv, oacc[7] * inv)};
  }
  __syncthreads();
  *(u32x4*)(O2 + ((size_t)hp * S_ + qb * 128) * 32 + tid * 8) = *(const u32x4*)(smem + tid * 16);
  asm volatile("" ::"v"(pfv[0]), "v"(pfv[1]), "v"(pfv[2]), "v"(pfv[3]), "v"(pfv[4]));
}

extern "C" void kernel_launch(void* const* d_in, const int* in_sizes, int n_in, void* d_out, int out_size,
                              void* d_ws, size_t ws_size, hipStream_t stream) {
  const u16* x = (const u16*)d_in[0];
  const u16* wqkv = (const u16*)d_in[1];
  const u16* wo = (const u16*)d_in[2];
  const u16* w_in = (const u16*)d_in[4];
  const u16* w_sp = (const u16*)d_in[5];
  const u16* b_sp = (const u16*)d_in[6];
  const u16* w_out = (const u16*)d_in[7];
  u16* out = (u16*)d_out;

  char* ws = (char*)d_ws;
  u16* Qb = (u16*)(ws);
  u16* Kb = (u16*)(ws + (2u << 20));
  u16* Vb = (u16*)(ws + (4u << 20));
  u16* Ob = (u16*)(ws + (6u << 20));
  u16* Hb = (u16*)(ws + (8u << 20));
  u16* Zb = (u16*)(ws + (10u << 20));
  u16* Ub = (u16*)(ws + (18u << 20));
  float* Pb = (float*)(ws + (22u << 20));

  gemm_k<128, 128, 4, 2, 1, 1, 8, 4, M_QKV, 512, 1536, 4, 6><<<256, 768, 0, stream>>>(
      x, wqkv, wo, w_in, nullptr, Qb, Kb, Vb, nullptr);
  attn_k<<<256, 512, 0, stream>>>(Qb, Kb, Vb, Ob, w_sp, w_out);
  gemm_k<64, 64, 2, 2, 2, 2, 4, 4, M_WO, 512, 512, 4, 8><<<256, 768, 0, stream>>>(
      Ob, wo, x, nullptr, nullptr, Hb, nullptr, nullptr, nullptr);
  gemm_k<128, 128, 4, 2, 1, 1, 8, 4, M_WIN, 512, 1792, 4, 7><<<224, 768, 0, stream>>>(
      Hb, w_in, nullptr, nullptr, nullptr, Zb, nullptr, nullptr, Pb);
  gemm_k<64, 128, 2, 1, 4, 2, 16, 3, M_SP, 2048, 1792, 4, 7><<<224, 768, 0, stream>>>(
      w_sp, Zb + FH_, Zb, b_sp, Pb, Ub, nullptr, nullptr, nullptr);
  gemm_k<64, 64, 2, 2, 2, 2, 7, 4, M_OUT, 896, 512, 4, 8><<<256, 768, 0, stream>>>(
      Ub, w_out, Hb, nullptr, nullptr, out, nullptr, nullptr, nullptr);
}
```

```cpp
#include <hip/hip_runtime.h>
#include <hip/hip_bf16.h>
#include <type_traits>

typedef unsigned short u16;
typedef short s16x8 __attribute__((ext_vector_type(8)));
typedef short s16x4 __attribute__((ext_vector_type(4)));
typedef float f32x4 __attribute__((ext_vector_type(4)));
typedef float f32x2 __attribute__((ext_vector_type(2)));
typedef float f32x16 __attribute__((ext_vector_type(16)));
typedef __bf16 bf16x2 __attribute__((ext_vector_type(2)));
typedef __bf16 bf16x8 __attribute__((ext_vector_type(8)));
typedef unsigned u32x4 __attribute__((ext_vector_type(4)));
typedef unsigned u32x2 __attribute__((ext_vector_type(2)));

#define DI __device__ __forceinline__
#define SCHED_FENCE __builtin_amdgcn_sched_barrier(0)

constexpr int S_ = 2048, D_ = 512, H_ = 32, DH_ = 16, F_ = 1792, FH_ = 896;

DI unsigned pack2(float a, float b) {
  bf16x2 v = {(__bf16)a, (__bf16)b};
  return __builtin_bit_cast(unsigned, v);
}
DI float bflo(unsigned u) { return __uint_as_float(u << 16); }
DI float bfhi(unsigned u) { return __uint_as_float(u & 0xffff0000u); }
DI float rbf(float a) {
  __bf16 v = (__bf16)a;
  return __uint_as_float(((unsigned)__builtin_bit_cast(u16, v)) << 16);
}
DI void ln_scalars(float sum, float sumsq, float inv_n, float& mb, float& r) {
  float mean = sum * inv_n;
  float var = fmaxf(sumsq * inv_n - mean * mean, 0.f);
  mb = rbf(mean);
  r = 1.f / rbf(sqrtf(rbf(rbf(var) + 1e-5f)));
}
DI float red4(float v) {
  unsigned u = __float_as_uint(v);
  auto r = __builtin_amdgcn_permlane16_swap(u, u, false, false);
  float a = __uint_as_float(r[0]) + __uint_as_float(r[1]);
  unsigned w = __float_as_uint(a);
  auto q = __builtin_amdgcn_permlane32_swap(w, w, false, false);
  return __uint_as_float(q[0]) + __uint_as_float(q[1]);
}
DI float rowsum8(float v) {
  v += __int_as_float(__builtin_amdgcn_update_dpp(0, __float_as_int(v), 0x111, 0xf, 0xf, true));
  v += __int_as_float(__builtin_amdgcn_update_dpp(0, __float_as_int(v), 0x112, 0xf, 0xf, true));
  v += __int_as_float(__builtin_amdgcn_update_dpp(0, __float_as_int(v), 0x114, 0xf, 0xf, true));
  return v;
}
DI float rowsum16(float v) {
  v += __int_as_float(__builtin_amdgcn_update_dpp(0, __float_as_int(v), 0x111, 0xf, 0xf, true));
  v += __int_as_float(__builtin_amdgcn_update_dpp(0, __float_as_int(v), 0x112, 0xf, 0xf, true));
  v += __int_as_float(__builtin_amdgcn_update_dpp(0, __float_as_int(v), 0x114, 0xf, 0xf, true));
  v += __int_as_float(__builtin_amdgcn_update_dpp(0, __float_as_int(v), 0x118, 0xf, 0xf, true));
  return v;
}
DI float dot2(unsigned a, unsigned b, float c) {
  return __builtin_amdgcn_fdot2_f32_bf16(__builtin_bit_cast(bf16x2, a), __builtin_bit_cast(bf16x2, b), c, false);
}
DI f32x4 mfma16(u32x4 a, u32x4 b, f32x4 c) {
  return __builtin_amdgcn_mfma_f32_16x16x32_bf16(__builtin_bit_cast(bf16x8, a), __builtin_bit_cast(bf16x8, b), c, 0, 0, 0);
}
DI f32x16 mfma32(u32x4 a, u32x4 b, f32x16 c) {
  return __builtin_amdgcn_mfma_f32_32x32x16_bf16(__builtin_bit_cast(bf16x8, a), __builtin_bit_cast(bf16x8, b), c, 0, 0, 0);
}
template <int N> DI void wait_vm() { asm volatile("s_waitcnt vmcnt(%0)" ::"n"(N) : "memory"); }
DI void wait_lgkm0() { asm volatile("s_waitcnt lgkmcnt(0)" ::: "memory"); }
DI void bar() {
  asm volatile("" ::: "memory");
  __builtin_amdgcn_s_barrier();
  asm volatile("" ::: "memory");
}

template <int OFF> DI u32x2 tr_read(unsigned addr) {
  u32x2 r;
  asm volatile("ds_read_b64_tr_b16 %0, %1 offset:%2" : "=v"(r) : "v"(addr), "n"(OFF));
  return r;
}
template <int OFF> DI u32x4 lds_read128(unsigned addr) {
  u32x4 r;
  asm volatile("ds_read_b128 %0, %1 offset:%2" : "=v"(r) : "v"(addr), "n"(OFF));
  return r;
}
template <int N> DI void wait_lgkm() { asm volatile("s_waitcnt lgkmcnt(%0)" ::"n"(N) : "memory"); }
template <int I, int N, class F> DI void static_for(F&& f) {
  if constexpr (I < N) {
    f(std::integral_constant<int, I>{});
    static_for<I + 1, N>(f);
  }
}
DI void glds16(const void* g, void* l) {
  __builtin_amdgcn_global_load_lds((const unsigned*)g, (unsigned*)l, 16, 0, 0);
}

template <int KT, int NS> constexpr int iss_before(int kt) {
  int is = 2;
  for (int k = 0; k < kt; ++k) {
    int n = is + 2;
    if (n > k + NS) n = k + NS;
    if (n > KT) n = KT;
    is = n;
  }
  return is;
}
enum { M_QKV = 0, M_WO = 1, M_WIN = 2, M_SP = 3, M_OUT = 4 };

template <int BM, int BN, int WM, int WN, int WK, int SUB, int KT, int NS, int MODE, int LDA, int LDB, int PM, int PN>
__global__ __launch_bounds__((WM * WN * WK + 4) * 64, (WM * WN * WK == 4) ? 2 : 3) void gemm_k(const u16* __restrict__ A, const u16* __restrict__ B,
                                                 const u16* __restrict__ e0, const u16* __restrict__ e1,
                                                 const float* __restrict__ partin, u16* __restrict__ o0,
                                                 u16* __restrict__ o1, u16* __restrict__ o2,
                                                 float* __restrict__ partout) {
  constexpr int NCW = WM * WN * WK;
  constexpr int NT = (NCW + 4) * 64;
  static_assert(NCW == 8 || NCW == 4, "consumer waves");
  constexpr int AB = BM * 128, BB = BN * 128, SUBB = AB + BB, STG = SUB * SUBB;
  constexpr int AI = AB / 1024, BI = BB / 1024;
  static_assert(AI % 4 == 0, "A instr");
  constexpr int LA = AI / 4;
  constexpr int LBF = BI / 4, LBR = BI % 4;
  constexpr int MB = BM / WM / 16, NB = BN / WN / 16;
  constexpr int RING = NS * STG;
  constexpr int EXTRA = (MODE == M_SP) ? 16384 : (NCW == 4 ? 1024 : 0);
  constexpr bool LNA = (MODE == M_QKV || MODE == M_WIN);
  __shared__ __attribute__((aligned(16))) char smem[RING + EXTRA];

  const int tid = threadIdx.x, lane = tid & 63;
  const int wid = __builtin_amdgcn_readfirstlane(tid >> 6);

  if constexpr (MODE == M_QKV) {
    if (blockIdx.x >= 192) {
      const int pb = blockIdx.x - 192;
      u32x4 accx = {0u, 0u, 0u, 0u};
      auto pf = [&](const u16* p, int n16) {
        const u32x4* q = (const u32x4*)p;
        for (int i = pb * NT + tid; i < n16; i += 64 * NT * 4) {
          u32x4 a = q[i];
          u32x4 b = (i + 64 * NT < n16) ? q[i + 64 * NT] : a;
          u32x4 c = (i + 2 * 64 * NT < n16) ? q[i + 2 * 64 * NT] : a;
          u32x4 d = (i + 3 * 64 * NT < n16) ? q[i + 3 * 64 * NT] : a;
          accx ^= a ^ b ^ c ^ d;
        }
      };
      pf(e0, 512 * 512 / 8);
      pf(e1, 512 * 1792 / 8);
      asm volatile("" ::"v"(accx));
      return;
    }
  }
  constexpr int TN = (MODE == M_QKV ? 1536 : MODE == M_WIN ? 1792 : MODE == M_SP ? 896 : 512) / BN;
  constexpr int PXN = TN / PN;
  const int bid = blockIdx.x, xcd = bid & 7, jj = bid >> 3;
  const int tm = (xcd / PXN) * PM + jj / PN, tn = (xcd % PXN) * PN + jj % PN;
  const int m0 = tm * BM, n0 = tn * BN;

  const bool is_loader = wid >= NCW;
  if (is_loader) {
    const int lw = wid - NCW;
    unsigned aoff[LA], boff[LBF + 1];
#pragma unroll
    for (int i = 0; i < LA; ++i) {
      int q = lw + 4 * i;
      int row = q * 8 + (lane >> 3), pos = lane & 7;
      int ch = pos ^ (row & 7);
      if constexpr (MODE == M_WO) aoff[i] = (unsigned)(((ch >> 2) * S_ + m0 + row) * 32 + (ch & 3) * 8);
      else aoff[i] = (unsigned)((m0 + row) * LDA + (ch << 3));
    }
#pragma unroll
    for (int i = 0; i < LBF; ++i) {
      int q = lw + 4 * i;
      int r = (q & 7) * 8 + (lane >> 3), pos = lane & 7;
      int h = ((r >> 1) & 1) | (((r >> 3) & 1) << 1);
      int ch = (((pos >> 1) ^ h) << 1) | (pos & 1);
      boff[i] = (unsigned)(r * LDB + n0 + (q >> 3) * 64 + ch * 8);
    }
    static_assert(LBR == 0, "BN % 32");
    const bool extraB = (lw < LBR);
    auto issue = [&](int kt, int slot) {
#pragma unroll
      for (int sb = 0; sb < SUB; ++sb) {
        const u16* a = A + (kt * SUB + sb) * (MODE == M_WO ? 2 * S_ * 32 : 64);
        const u16* b = B + (size_t)(kt * SUB + sb) * 64 * LDB;
        char* s = smem + slot * STG + sb * SUBB;
#pragma unroll
        for (int i = 0; i < LA; ++i) glds16(a + aoff[i], s + (lw + 4 * i) * 1024);
#pragma unroll
        for (int i = 0; i < LBF; ++i) glds16(b + boff[i], s + AB + (lw + 4 * i) * 1024);
        if (LBR != 0) {
          if (extraB) glds16(b + boff[LBF], s + AB + (lw + 4 * LBF) * 1024);
        }
      }
    };
    issue(0, 0);
    issue(1, 1);
    float ls1 = 0.f, ls2 = 0.f;
    const unsigned lrd = (unsigned)(size_t)smem + (lw * 32 + (lane >> 1)) * 128 + (lane & 1) * 64;
    static_for<0, KT>([&](auto KTI) {
      constexpr int kt = KTI;
      constexpr int is0 = iss_before<KT, NS>(kt);
      constexpr int is1 = iss_before<KT, NS>(kt + 1);
      constexpr int later = is0 - 1 - kt;
      if (LBR != 0 && extraB) wait_vm<later * SUB * (LA + LBF + 1)>();
      else wait_vm<later * SUB * (LA + LBF)>();
      bar();
      if constexpr (NCW == 4 && MODE == M_QKV) {
        constexpr int so = (kt % NS) * STG;
        u32x4 v0 = lds_read128<(so + 0) & 0xffff>(lrd + (so & ~0xffff));
        u32x4 v1 = lds_read128<(so + 16) & 0xffff>(lrd + (so & ~0xffff));
        u32x4 v2 = lds_read128<(so + 32) & 0xffff>(lrd + (so & ~0xffff));
        u32x4 v3 = lds_read128<(so + 48) & 0xffff>(lrd + (so & ~0xffff));
        if constexpr (is1 > is0) issue(is0, is0 % NS);
        if constexpr (is1 > is0 + 1) issue(is0 + 1, (is0 + 1) % NS);
        wait_lgkm0();
        asm volatile("" : "+v"(v0), "+v"(v1), "+v"(v2), "+v"(v3));
#pragma unroll
        for (int e = 0; e < 4; ++e) {
          ls1 = dot2(v0[e], 0x3F803F80u, ls1); ls2 = dot2(v0[e], v0[e], ls2);
          ls1 = dot2(v1[e], 0x3F803F80u, ls1); ls2 = dot2(v1[e], v1[e], ls2);
          ls1 = dot2(v2[e], 0x3F803F80u, ls1); ls2 = dot2(v2[e], v2[e], ls2);
          ls1 = dot2(v3[e], 0x3F803F80u, ls1); ls2 = dot2(v3[e], v3[e], ls2);
        }
      } else {
        if constexpr (is1 > is0) issue(is0, is0 % NS);
        if constexpr (is1 > is0 + 1) issue(is0 + 1, (is0 + 1) % NS);
      }
    });
    if constexpr (NCW == 8) return;
    else if constexpr (MODE == M_QKV) {
      ls1 += __int_as_float(__builtin_amdgcn_update_dpp(0, __float_as_int(ls1), 0xB1, 0xf, 0xf, true));
      ls2 += __int_as_float(__builtin_amdgcn_update_dpp(0, __float_as_int(ls2), 0xB1, 0xf, 0xf, true));
      float mb, rs;
      ln_scalars(ls1, ls2, 1.f / 512.f, mb, rs);
      if ((lane & 1) == 0) *(f32x2*)(smem + RING + (lw * 32 + (lane >> 1)) * 8) = (f32x2){mb, rs};
      if constexpr (MODE == M_QKV) {
        wait_lgkm0();
        bar();
        return;
      }
    }
  }

  const int wk = wid % WK;
  const int wmn = wid / WK;
  const int wm = wmn / WN, wn = wmn % WN;
  const int r16 = lane & 15, g = lane >> 4;
  const int tokb = m0 + wm * (BM / WM);
  const int nbase = n0 + wn * (BN / WN);

  f32x2 hsum = {0.f, 0.f};
  if constexpr (MODE == M_WIN && NCW == 4) {
    if (tid >= 128 && tid < 128 + BM) {
#pragma unroll
      for (int p = 0; p < 8; ++p) hsum += *(const f32x2*)(partin + (size_t)(p * 2048 + m0 + tid - 128) * 2);
    }
  }
  if constexpr (MODE == M_SP) {
    float* sr = (float*)(smem + RING);
    float* srm = sr + 2048;
#pragma unroll
    for (int j = 0; j < 2; ++j) {
      int row = 2 * tid + 1024 * j;
      f32x4 t = {0.f, 0.f, 0.f, 0.f};
#pragma unroll
      for (int p = 0; p < 7; ++p) {
        f32x4 v = *(const f32x4*)(partin + (size_t)(p * 2048 + row) * 2);
        t += v;
      }
      float mean0, mean1, r0, r1;
      ln_scalars(t[0], t[1], 1.f / 896.f, mean0, r0);
      ln_scalars(t[2], t[3], 1.f / 896.f, mean1, r1);
      *(f32x2*)(sr + row) = (f32x2){r0, r1};
      *(f32x2*)(srm + row) = (f32x2){r0 * mean0, r1 * mean1};
    }
  }

  f32x4 acc[MB][NB];
#pragma unroll
  for (int i = 0; i < MB; ++i)
#pragma unroll
    for (int j = 0; j < NB; ++j) acc[i][j] = (f32x4){0.f, 0.f, 0.f, 0.f};
  float s1[MB], s2[MB], cs[NB];
  f32x2 cc2[MB];
#pragma unroll
  for (int i = 0; i < MB; ++i) { s1[i] = 0.f; s2[i] = 0.f; cc2[i] = (f32x2){0.f, 0.f}; }
#pragma unroll
  for (int j = 0; j < NB; ++j) cs[j] = 0.f;

  constexpr int NKS = 2 * SUB / WK;
  static_assert(NKS >= 1 && NKS <= 2, "NKS");
  static_assert(NKS == 1 || WK <= 2, "NKS");
  const unsigned lbase = (unsigned)(size_t)smem;
  unsigned ard[NKS], bpo[4];
  {
    int q = (lane & 15) >> 2, p = lane & 3;
    int row = wm * (BM / WM) + r16;
#pragma unroll
    for (int kq = 0; kq < NKS; ++kq) {
      int kk = wk + WK * kq;
      int sub = kk >> 1, ks = kk & 1;
      ard[kq] = lbase + sub * SUBB + row * 128 + (((4 * ks + g) ^ (row & 7)) << 4);
    }
    int h = ((q >> 1) & 1) | ((g & 1) << 1);
    int kk0 = wk;
    unsigned bb = lbase + (kk0 >> 1) * SUBB + AB + ((kk0 & 1) * 32 + 8 * g + q) * 128 + p * 8;
    constexpr int NBW = BN / WN / 16;
    static_assert(NBW % 4 == 0 || NBW == 2, "wave n-blocks");
    int nb0 = wn * NBW;
#pragma unroll
    for (int pc = 0; pc < 4; ++pc) {
      int nb = nb0 + pc;
      bpo[pc] = bb + (nb >> 2) * 8192 + (((nb & 3) ^ h) << 5);
    }
  }
  unsigned strd = lbase + RING + ((wk & 1) * 32 + (wk >> 1) * 64 + g * 8) * 4;

  constexpr int ST = (MODE == M_SP) ? 4 : 0;
  constexpr int PER = ST + MB + 2 * NB;
  constexpr int RTOT = NKS * PER;
  constexpr int NB0 = (NB + 1) / 2;

  f32x4 csa[(NCW == 4) ? 4 : 1];
  if constexpr (NCW == 4) {
#pragma unroll
    for (int j = 0; j < 4; ++j) csa[j] = (f32x4){0.f, 0.f, 0.f, 0.f};
    if (!is_loader) {
      static_assert(LNA && WN == 2 && WM == 2 && WK == 1 && MB == 4 && NB == 4 && NKS == 2 && SUB == 1, "pipelined path");
      struct Frags {
        u32x4 af[2][4];
        u32x2 bl[2][4], bh[2][4];
      };
      const u32x4 ones = {0x3F803F80u, 0x3F803F80u, 0x3F803F80u, 0x3F803F80u};
      auto PIN = [&](Frags& F) {
#pragma unroll
        for (int kq = 0; kq < 2; ++kq) {
#pragma unroll
          for (int i = 0; i < 4; ++i) asm volatile("" : "+v"(F.af[kq][i]));
#pragma unroll
          for (int j = 0; j < 4; ++j) asm volatile("" : "+v"(F.bl[kq][j]), "+v"(F.bh[kq][j]));
        }
      };
      auto RDG = [&](Frags& N, auto G, auto SO) {
        constexpr int kq = G >> 2, j = G & 3, so = SO;
        constexpr int oa = so + j * 2048, o1 = so + kq * 4096, o2 = so + kq * 4096 + 512;
        N.af[kq][j] = lds_read128<oa & 0xffff>(ard[kq] + (oa & ~0xffff));
        N.bl[kq][j] = tr_read<o1 & 0xffff>(bpo[j] + (o1 & ~0xffff));
        N.bh[kq][j] = tr_read<o2 & 0xffff>(bpo[j] + (o2 & ~0xffff));
      };
      Frags F[2];
      static_for<0, KT + 1>([&](auto KTI) {
        constexpr int kt = KTI;
        wait_lgkm0();
        if constexpr (kt < KT) bar();
        if constexpr (kt > 0) PIN(F[(kt - 1) & 1]);
        static_for<0, 8>([&](auto G) {
          constexpr int kq = G >> 2, j = G & 3;
          if constexpr (kt > 0) {
            Frags& C = F[(kt - 1) & 1];
            u32x4 b = {C.bl[kq][j][0], C.bl[kq][j][1], C.bh[kq][j][0], C.bh[kq][j][1]};
#pragma unroll
            for (int i = 0; i < 4; ++i) acc[i][j] = mfma16(b, C.af[kq][i], acc[i][j]);
            if (wm == kq) csa[j] = mfma16(b, ones, csa[j]);
          }
          if constexpr (kt < KT) RDG(F[kt & 1], G, std::integral_constant<int, (kt % NS) * STG>{});
          SCHED_FENCE;
        });
      });
    }
  } else {
  int slot = 0;
#pragma unroll 1
  for (int kt = 0; kt < KT; ++kt) {
    wait_lgkm0();
    bar();
    const unsigned so = slot * STG;
    u32x4 af[NKS][MB];
    u32x2 bl[NKS][NB], bh[NKS][NB];
    u32x4 st[4];
    static_for<0, NKS>([&](auto KQ) {
      if constexpr (MODE == M_SP) {
        st[0] = lds_read128<0>(strd);
        st[1] = lds_read128<16>(strd);
        st[2] = lds_read128<8192>(strd);
        st[3] = lds_read128<8208>(strd);
      }
      static_for<0, MB>([&](auto I) { af[KQ][I] = lds_read128<I * 2048>(ard[KQ] + so); });
      static_for<0, NB>([&](auto J) {
        constexpr int off = KQ * (WK == 1 ? 4096 : SUBB) + (J >> 2) * 8192;
        bl[KQ][J] = tr_read<off>(bpo[J & 3] + so);
        bh[KQ][J] = tr_read<off + 512>(bpo[J & 3] + so);
      });
    });
    if constexpr (MODE == M_SP) strd += SUB * 256;
    const bool docs = LNA && ((kt % WM) == wm);
    static_for<0, NKS>([&](auto KQ) {
      constexpr int w0 = RTOT - (KQ * PER + ST + MB + 2 * NB0);
      constexpr int w1 = RTOT - (KQ + 1) * PER;
      constexpr int wa = RTOT - (KQ * PER + ST + MB);
      wait_lgkm<(wa > 15 ? 15 : wa)>();
      u32x4 a[MB];
#pragma unroll
      for (int i = 0; i < MB; ++i) {
        asm volatile("" : "+v"(af[KQ][i]));
        a[i] = af[KQ][i];
      }
      if constexpr (MODE == M_SP) {
        asm volatile("" : "+v"(st[0]), "+v"(st[1]), "+v"(st[2]), "+v"(st[3]));
        f32x4 r0 = __builtin_bit_cast(f32x4, st[0]), r1 = __builtin_bit_cast(f32x4, st[1]);
        f32x4 m0v = __builtin_bit_cast(f32x4, st[2]), m1v = __builtin_bit_cast(f32x4, st[3]);
#pragma unroll
        for (int i = 0; i < MB; ++i) {
          f32x2 w01 = {bflo(a[i][0]), bfhi(a[i][0])}, w23 = {bflo(a[i][1]), bfhi(a[i][1])};
          f32x2 w45 = {bflo(a[i][2]), bfhi(a[i][2])}, w67 = {bflo(a[i][3]), bfhi(a[i][3])};
          f32x2 c = cc2[i];
          c = __builtin_elementwise_fma(w01, (f32x2){m0v[0], m0v[1]}, c);
          c = __builtin_elementwise_fma(w23, (f32x2){m0v[2], m0v[3]}, c);
          c = __builtin_elementwise_fma(w45, (f32x2){m1v[0], m1v[1]}, c);
          c = __builtin_elementwise_fma(w67, (f32x2){m1v[2], m1v[3]}, c);
          cc2[i] = c;
          w01 *= (f32x2){r0[0], r0[1]}; w23 *= (f32x2){r0[2], r0[3]};
          w45 *= (f32x2){r1[0], r1[1]}; w67 *= (f32x2){r1[2], r1[3]};
          a[i][0] = pack2(w01[0], w01[1]);
          a[i][1] = pack2(w23[0], w23[1]);
          a[i][2] = pack2(w45[0], w45[1]);
          a[i][3] = pack2(w67[0], w67[1]);
        }
      }
      if constexpr (LNA) {
#pragma unroll
        for (int i = 0; i < MB; ++i) {
          if constexpr (WN == 2) {
#pragma unroll
            for (int e = 0; e < 4; ++e) s1[i] = dot2(a[i][e], wn ? a[i][e] : 0x3F803F80u, s1[i]);
          } else {
#pragma unroll
            for (int e = 0; e < 4; ++e) {
              s1[i] = dot2(a[i][e], 0x3F803F80u, s1[i]);
              s2[i] = dot2(a[i][e], a[i][e], s2[i]);
            }
          }
        }
      }
#pragma unroll
      for (int i = 0; i < MB; ++i) asm volatile("" : "+v"(a[i]));
      wait_lgkm<(w0 > 15 ? 15 : w0)>();
#pragma unroll
      for (int j = 0; j < NB0; ++j) {
        asm volatile("" : "+v"(bl[KQ][j]), "+v"(bh[KQ][j]));
        u32x4 b = {bl[KQ][j][0], bl[KQ][j][1], bh[KQ][j][0], bh[KQ][j][1]};
#pragma unroll
        for (int i = 0; i < MB; ++i) acc[i][j] = mfma16(b, a[i], acc[i][j]);
      }
      wait_lgkm<(w1 > 15 ? 15 : w1)>();
#pragma unroll
      for (int j = NB0; j < NB; ++j) {
        asm volatile("" : "+v"(bl[KQ][j]), "+v"(bh[KQ][j]));
        u32x4 b = {bl[KQ][j][0], bl[KQ][j][1], bh[KQ][j][0], bh[KQ][j][1]};
#pragma unroll
        for (int i = 0; i < MB; ++i) acc[i][j] = mfma16(b, a[i], acc[i][j]);
      }
      if constexpr (LNA) {
        if (docs) {
#pragma unroll
          for (int j = 0; j < NB; ++j) {
            cs[j] = dot2(bl[KQ][j][0], 0x3F803F80u, cs[j]);
            cs[j] = dot2(bl[KQ][j][1], 0x3F803F80u, cs[j]);
            cs[j] = dot2(bh[KQ][j][0], 0x3F803F80u, cs[j]);
            cs[j] = dot2(bh[KQ][j][1], 0x3F803F80u, cs[j]);
          }
        }
      }
    });
    ++slot; if (slot == NS) slot = 0;
  }

  }

  if constexpr (MODE != M_QKV) {
    constexpr int RSF = BN * 4 + 16;
    constexpr int PLB = BM * RSF;
    constexpr int O_ROWP = WK * PLB;
    constexpr int O_COLP = O_ROWP + WK * BM * 8;
    constexpr int O_ROWF = O_COLP + 8 * BN * 4;
    constexpr int O_COLF = O_ROWF + BM * 8;
    static_assert(O_COLF + BN * 4 <= RING, "epilogue LDS");
    constexpr int CH = BN / 8;
    constexpr int LPR = (CH <= 8) ? 8 : 16;
    constexpr int RPP = 512 / LPR;
    constexpr int NPASS = BM / RPP;
    const int ec = tid & (LPR - 1), er = tid / LPR;
    const bool eact = ec < CH;
    u32x4 eop[NPASS];
    if constexpr (MODE != M_WIN) {
      constexpr int LDE = (MODE == M_SP) ? F_ : D_;
#pragma unroll
      for (int ps = 0; ps < NPASS; ++ps)
        if (eact) eop[ps] = *(const u32x4*)(e0 + (size_t)(m0 + er + ps * RPP) * LDE + n0 + ec * 8);
    }
    wait_lgkm0();
    bar();
    if (!is_loader) {
      char* pl = smem + wk * PLB + (wm * (BM / WM) + r16) * RSF + (wn * (BN / WN) + 4 * g) * 4;
#pragma unroll
      for (int i = 0; i < MB; ++i)
#pragma unroll
        for (int j = 0; j < NB; ++j) *(f32x4*)(pl + i * 16 * RSF + j * 64) = acc[i][j];
    }
    if constexpr ((MODE == M_WIN && NCW == 8) || MODE == M_SP) {
#pragma unroll
      for (int i = 0; i < MB; ++i) {
        if constexpr (MODE == M_WIN && WN == 2) {
          float a = red4(s1[i]);
          if (g == 0) *(float*)(smem + O_ROWP + (wk * BM + wm * (BM / WM) + i * 16 + r16) * 8 + wn * 4) = a;
        } else {
          float a = (MODE == M_WIN) ? red4(s1[i]) : red4(cc2[i][0] + cc2[i][1]);
          float b = (MODE == M_WIN) ? red4(s2[i]) : 0.f;
          if (g == 0) *(f32x2*)(smem + O_ROWP + (wk * BM + wm * (BM / WM) + i * 16 + r16) * 8) = (f32x2){a, b};
        }
      }
    }
    if constexpr (MODE == M_WIN && NCW == 4) {
      if (!is_loader && r16 == 0) {
#pragma unroll
        for (int j = 0; j < NB; ++j) *(f32x4*)(smem + O_COLP + (wm * BN + wn * (BN / WN) + j * 16 + 4 * g) * 4) = csa[j];
      }
    }
    if constexpr (MODE == M_WIN && NCW == 8) {
#pragma unroll
      for (int j = 0; j < NB; ++j) {
        float v = red4(cs[j]);
        if (g == 0) *(float*)(smem + O_COLP + ((wm * WK + wk) * BN + wn * (BN / WN) + j * 16 + r16) * 4) = v;
      }
    }
    wait_lgkm0();
    bar();
    if constexpr (MODE == M_WIN) {
      if (tid < BN) {
        float c = 0.f;
#pragma unroll
        for (int w = 0; w < WM * WK; ++w) c += *(const float*)(smem + O_COLP + (w * BN + tid) * 4);
        *(float*)(smem + O_COLF + tid * 4) = c;
      } else if (NCW == 4 && tid >= 128 && tid < 128 + BM) {
        int row = tid - 128;
        float mean, rs;
        ln_scalars(hsum[0], hsum[1], 1.f / 512.f, mean, rs);
        *(f32x2*)(smem + O_ROWF + row * 8) = (f32x2){mean, rs};
      } else if (tid >= 128 && tid < 128 + BM) {
        int row = tid - 128;
        f32x2 t = {0.f, 0.f};
#pragma unroll
        for (int k = 0; k < WK; ++k) t += *(const f32x2*)(smem + O_ROWP + (k * BM + row) * 8);
        float mean, rs;
        ln_scalars(t[0], t[1], 1.f / 512.f, mean, rs);
        *(f32x2*)(smem + O_ROWF + row * 8) = (f32x2){mean, rs};
      }
      wait_lgkm0();
      bar();
    }
    f32x4 cf0 = {0.f, 0.f, 0.f, 0.f}, cf1 = cf0;
    if constexpr (MODE == M_WIN) {
      if (eact) {
        cf0 = *(const f32x4*)(smem + O_COLF + ec * 32);
        cf1 = *(const f32x4*)(smem + O_COLF + ec * 32 + 16);
      }
    }
#pragma unroll
    for (int ps = 0; ps < NPASS; ++ps) {
      const int row = er + ps * RPP;
      const int tok = m0 + row;
      float st1 = 0.f, st2 = 0.f;
      if (eact) {
        f32x4 v0 = *(const f32x4*)(smem + row * RSF + ec * 32);
        f32x4 v1 = *(const f32x4*)(smem + row * RSF + ec * 32 + 16);
#pragma unroll
        for (int k = 1; k < WK; ++k) {
          v0 += *(const f32x4*)(smem + k * PLB + row * RSF + ec * 32);
          v1 += *(const f32x4*)(smem + k * PLB + row * RSF + ec * 32 + 16);
        }
        float v[8] = {v0[0], v0[1], v0[2], v0[3], v1[0], v1[1], v1[2], v1[3]};
        u32x4 ov;
        if constexpr (MODE == M_WO || MODE == M_OUT) {
#pragma unroll
          for (int e = 0; e < 4; ++e)
            ov[e] = pack2(bflo(eop[ps][e]) + rbf(v[2 * e]), bfhi(eop[ps][e]) + rbf(v[2 * e + 1]));
          *(u32x4*)(o0 + (size_t)tok * D_ + n0 + ec * 8) = ov;
          if constexpr (MODE == M_WO) {
#pragma unroll
            for (int e = 0; e < 4; ++e) {
              st1 = dot2(ov[e], 0x3F803F80u, st1);
              st2 = dot2(ov[e], ov[e], st2);
            }
          }
        } else if constexpr (MODE == M_SP) {
          float c = 0.f;
#pragma unroll
          for (int k = 0; k < WK; ++k) c += *(const float*)(smem + O_ROWP + (k * BM + row) * 8);
          const float bias = bflo((unsigned)e1[tok]);
#pragma unroll
          for (int e = 0; e < 4; ++e) {
            float g0 = rbf(rbf(v[2 * e] - c) + bias), g1 = rbf(rbf(v[2 * e + 1] - c) + bias);
            ov[e] = pack2(bflo(eop[ps][e]) * g0, bfhi(eop[ps][e]) * g1);
          }
          *(u32x4*)(o0 + (size_t)tok * FH_ + n0 + ec * 8) = ov;
        } else {
          f32x2 ms = *(const f32x2*)(smem + O_ROWF + row * 8);
          float cf[8] = {cf0[0], cf0[1], cf0[2], cf0[3], cf1[0], cf1[1], cf1[2], cf1[3]};
          float zz[8];
#pragma unroll
          for (int e = 0; e < 8; ++e) {
            float x = rbf(ms[1] * (v[e] - ms[0] * cf[e]));
            float a = x * fmaf(x * x, -0.10294324f, -2.3022082f);
            float t = __builtin_amdgcn_exp2f(a);
            zz[e] = x * __builtin_amdgcn_rcpf(1.f + t);
          }
#pragma unroll
          for (int e = 0; e < 4; ++e) ov[e] = pack2(zz[2 * e], zz[2 * e + 1]);
          *(u32x4*)(o0 + (size_t)tok * F_ + n0 + ec * 8) = ov;
          if (n0 >= FH_) {
#pragma unroll
            for (int e = 0; e < 4; ++e) {
              st1 = dot2(ov[e], 0x3F803F80u, st1);
              st2 = dot2(ov[e], ov[e], st2);
            }
          }
        }
      }
      if constexpr (MODE == M_WO) {
        static_assert(LPR == 8, "row reduce");
        st1 = rowsum8(st1);
        st2 = rowsum8(st2);
        if (ec == 7) *(f32x2*)(partout + (size_t)(tn * 2048 + tok) * 2) = (f32x2){st1, st2};
      }
      if constexpr (MODE == M_WIN) {
        if (n0 >= FH_) {
          static_assert(LPR == 16, "row reduce");
          st1 = rowsum16(st1);
          st2 = rowsum16(st2);
          if (ec == 15) *(f32x2*)(partout + (size_t)(((n0 - FH_) / BN) * 2048 + tok) * 2) = (f32x2){st1, st2};
        }
      }
    }
    return;
  }
  if constexpr (MODE == M_QKV) {
    static_assert(WK == 1 && WN == 2 && MB == 4 && NCW == 4, "qkv epilogue");
    wait_lgkm0();
    bar();
    f32x4* ex = (f32x4*)smem;
#pragma unroll
    for (int j = 0; j < NB; ++j) ex[(wid * 4 + j) * 64 + lane] = csa[j];
    float mean[MB], rstd[MB];
#pragma unroll
    for (int i = 0; i < MB; ++i) {
      f32x2 ms = *(const f32x2*)(smem + RING + (wm * 64 + i * 16 + r16) * 8);
      mean[i] = ms[0]; rstd[i] = ms[1];
    }
    wait_lgkm0();
    bar();
    const int which = n0 >> 9;
#pragma unroll
    for (int j = 0; j < NB; ++j) {
      f32x4 c4 = csa[j] + ex[((wid ^ 2) * 4 + j) * 64 + lane];
      const int n = nbase + j * 16;
      const int head = (n & 511) >> 4;
      if (which < 2) {
        u16* dst = (which == 0 ? o0 : o1);
        const float sc = (which == 0) ? 0.36067376022224085f : 1.f;
#pragma unroll
        for (int i = 0; i < MB; ++i) {
          int tok = tokb + i * 16 + r16;
          float rs = rstd[i] * sc;
          float v0 = rs * (acc[i][j][0] - mean[i] * c4[0]);
          float v1 = rs * (acc[i][j][1] - mean[i] * c4[1]);
          float v2 = rs * (acc[i][j][2] - mean[i] * c4[2]);
          float v3 = rs * (acc[i][j][3] - mean[i] * c4[3]);
          *(u32x2*)(dst + ((size_t)head * S_ + tok) * 16 + 4 * g) = (u32x2){pack2(v0, v1), pack2(v2, v3)};
        }
      } else {
        u16* vt = (u16*)(smem + 16384 + (wid * NB + j) * 2048);
#pragma unroll
        for (int i = 0; i < MB; ++i) {
#pragma unroll
          for (int e = 0; e < 4; ++e) {
            float v = rstd[i] * (acc[i][j][e] - mean[i] * c4[e]);
            __bf16 bv = (__bf16)v;
            vt[(4 * g + e) * 64 + i * 16 + r16] = __builtin_bit_cast(u16, bv);
          }
        }
#pragma unroll
        for (int hf = 0; hf < 2; ++hf) {
          int d = hf * 8 + (lane >> 3), c = lane & 7;
          u32x4 v = *(const u32x4*)(vt + d * 64 + c * 8);
          *(u32x4*)(o2 + ((size_t)head * 16 + d) * S_ + tokb + c * 8) = v;
        }
      }
    }
  }
}

constexpr int AT_KB = 384 * 32;
constexpr int AT_VS = 784;
constexpr int AT_VB = 18 * AT_VS;
constexpr int AT_QB = 128 * 32;
constexpr int AT_HB = AT_KB + AT_VB + AT_QB;
__global__ __launch_bounds__(512, 2) void attn_k(const u16* __restrict__ Q, const u16* __restrict__ K,
                                                 const u16* __restrict__ Vt, u16* __restrict__ O2,
                                                 const u16* __restrict__ pf0, const u16* __restrict__ pf1) {
  __shared__ __attribute__((aligned(16))) char smem[2 * AT_HB];
  const int tid = threadIdx.x, lane = tid & 63;
  const int wid = __builtin_amdgcn_readfirstlane(tid >> 6);
  const int hp = blockIdx.x >> 4, qb = blockIdx.x & 15;
  const int hl = wid >> 2;
  const int head = hp * 2 + hl;
  const int qt = qb * 4 + (wid & 3);
  const int q0 = qt * 32;
  const int kw0 = qb * 128 - 256;
  const int i = lane & 31, hh = lane >> 5;

  {
    const int w4 = wid & 3;
    const u16* Qh = Q + ((size_t)head * S_ + qb * 128) * 16;
    const u16* Kh = K + (size_t)head * S_ * 16;
    const u16* Vh = Vt + (size_t)head * 16 * S_;
    char* kl = smem + hl * AT_HB;
    char* vl = kl + AT_KB;
    char* ql = vl + AT_VB;
    glds16(Qh + (w4 * 64 + lane) * 8, ql + w4 * 1024);
#pragma unroll
    for (int r = 0; r < 3; ++r) {
      int c = (w4 * 3 + r) * 64 + lane;
      int row = c >> 1;
      int key = kw0 + row; key = key < 0 ? 0 : key;
      glds16(Kh + key * 16 + (((c & 1) ^ ((row >> 3) & 1)) << 3), kl + (w4 * 3 + r) * 1024);
    }
    {
      int k2 = kw0 + lane * 8; k2 = k2 < 0 ? 0 : k2;
      if (lane < 48) {
#pragma unroll
        for (int r = 0; r < 4; ++r) glds16(Vh + (w4 * 4 + r) * S_ + k2, vl + (w4 * 4 + r) * AT_VS);
      }
    }
    const int t = tid & 255;
    if (t < 96) {
      int d = t / 48, cc = t % 48;
      unsigned f = (d == 0) ? 0x3F803F80u : 0u;
      *(u32x4*)(vl + (16 + d) * AT_VS + cc * 16) = (u32x4){f, f, f, f};
    }
  }

  const int t0 = (q0 >= 256) ? 0 : (8 - qt);
  const int pi = ((i >> 2) & 1) * 16 + ((i >> 4) & 1) * 8 + ((i >> 3) & 1) * 4 + (i & 3);
  const float NEG = -1e30f;
  f32x16 c0, c8, zero;
#pragma unroll
  for (int r = 0; r < 16; ++r) {
    int kap = 16 * hh + r;
    zero[r] = 0.f;
    c0[r] = (kap > i) ? 0.f : NEG;
    c8[r] = (kap <= i) ? 0.f : NEG;
  }
  wait_vm<4>();
  wait_lgkm0();
  bar();
  const int wrow = (wid & 3) * 32;
  const char* kr = smem + hl * AT_HB + (wrow + pi) * 32 + ((hh ^ ((pi >> 3) & 1)) << 4);
  const char* vr = smem + hl * AT_HB + AT_KB + (i < 17 ? i : 17) * AT_VS + (wrow + 16 * hh) * 2;

  const u32x4 qf = *(const u32x4*)(smem + hl * AT_HB + AT_KB + AT_VB + (wrow + i) * 32 + hh * 16);
  u32x4 kf[9];
#pragma unroll
  for (int t = 0; t < 9; ++t) kf[t] = *(const u32x4*)(kr + t * 1024);

  float mx = NEG;
#pragma unroll
  for (int t = 0; t < 9; ++t) {
    if (t >= t0) {
      f32x16 s = mfma32(kf[t], qf, t == 0 ? c0 : (t == 8 ? c8 : zero));
#pragma unroll
      for (int r = 0; r < 16; r += 2) mx = fmaxf(fmaxf(mx, s[r]), s[r + 1]);
    }
  }
  {
    unsigned u = __float_as_uint(mx);
    auto r = __builtin_amdgcn_permlane32_swap(u, u, false, false);
    mx = fmaxf(__uint_as_float(r[0]), __uint_as_float(r[1]));
  }
  f32x16 cm;
#pragma unroll
  for (int r = 0; r < 16; ++r) { cm[r] = -mx; c0[r] -= mx; c8[r] -= mx; }
  wait_vm<0>();
  bar();
  u32x4 pfv[5];
  {
    const u32x4* q0p = (const u32x4*)pf0;
    const int gi = blockIdx.x * 512 + tid;
#pragma unroll
    for (int r = 0; r < 4; ++r) pfv[r] = q0p[gi + r * 131072];
    pfv[4] = ((const u32x4*)pf1)[gi < 57344 ? gi : 0];
  }


  f32x16 oacc = zero;
#pragma unroll
  for (int t = 0; t < 9; ++t) {
    if (t >= t0) {
      u32x4 v0 = *(const u32x4*)(vr + t * 64);
      u32x4 v1 = *(const u32x4*)(vr + t * 64 + 16);
      f32x16 s = mfma32(kf[t], qf, t == 0 ? c0 : (t == 8 ? c8 : cm));
      float p[16];
#pragma unroll
      for (int r = 0; r < 16; ++r) p[r] = __builtin_amdgcn_exp2f(s[r]);
      u32x4 p0 = {pack2(p[0], p[1]), pack2(p[2], p[3]), pack2(p[4], p[5]), pack2(p[6], p[7])};
      u32x4 p1 = {pack2(p[8], p[9]), pack2(p[10], p[11]), pack2(p[12], p[13]), pack2(p[14], p[15])};
      oacc = mfma32(v0, p0, oacc);
      oacc = mfma32(v1, p1, oacc);
    }
  }
  float l;
  {
    unsigned u = __float_as_uint(oacc[8]);
    auto r = __builtin_amdgcn_permlane32_swap(u, u, false, false);
    l = __uint_as_float(r[0]) + __uint_as_float(r[1]);
  }
  float inv = 1.f / l;
  __syncthreads();
  {
    char* ot = smem + ((wid & 3) * 32 + i) * 64 + hl * 32 + hh * 8;
    *(u32x2*)(ot) = (u32x2){pack2(oacc[0] * inv, oacc[1] * inv), pack2(oacc[2] * inv, oacc[3] * inv)};
    *(u32x2*)(ot + 16) = (u32x2){pack2(oacc[4] * inv, oacc[5] * inv), pack2(oacc[6] * inv, oacc[7] * inv)};
  }
  __syncthreads();
  *(u32x4*)(O2 + ((size_t)hp * S_ + qb * 128) * 32 + tid * 8) = *(const u32x4*)(smem + tid * 16);
  asm volatile("" ::"v"(pfv[0]), "v"(pfv[1]), "v"(pfv[2]), "v"(pfv[3]), "v"(pfv[4]));
}

extern "C" void kernel_launch(void* const* d_in, const int* in_sizes, int n_in, void* d_out, int out_size,
                              void* d_ws, size_t ws_size, hipStream_t stream) {
  const u16* x = (const u16*)d_in[0];
  const u16* wqkv = (const u16*)d_in[1];
  const u16* wo = (const u16*)d_in[2];
  const u16* w_in = (const u16*)d_in[4];
  const u16* w_sp = (const u16*)d_in[5];
  const u16* b_sp = (const u16*)d_in[6];
  const u16* w_out = (const u16*)d_in[7];
  u16* out = (u16*)d_out;

  char* ws = (char*)d_ws;
  u16* Qb = (u16*)(ws);
  u16* Kb = (u16*)(ws + (2u << 20));
  u16* Vb = (u16*)(ws + (4u << 20));
  u16* Ob = (u16*)(ws + (6u << 20));
  u16* Hb = (u16*)(ws + (8u << 20));
  u16* Zb = (u16*)(ws + (10u << 20));
  u16* Ub = (u16*)(ws + (18u << 20));
  float* Pb = (float*)(ws + (22u << 20));
  float* Ph = (float*)(ws + (22u << 20) + (256u << 10));

  gemm_k<128, 128, 2, 2, 1, 1, 8, 4, M_QKV, 512, 1536, 4, 6><<<256, 512, 0, stream>>>(
      x, wqkv, wo, w_in, nullptr, Qb, Kb, Vb, nullptr);
  attn_k<<<256, 512, 0, stream>>>(Qb, Kb, Vb, Ob, w_sp, w_out);
  gemm_k<64, 64, 2, 2, 2, 2, 4, 4, M_WO, 512, 512, 4, 8><<<256, 768, 0, stream>>>(
      Ob, wo, x, nullptr, nullptr, Hb, nullptr, nullptr, Ph);
  gemm_k<128, 128, 2, 2, 1, 1, 8, 4, M_WIN, 512, 1792, 4, 7><<<224, 512, 0, stream>>>(
      Hb, w_in, nullptr, nullptr, Ph, Zb, nullptr, nullptr, Pb);
  gemm_k<64, 128, 2, 1, 4, 2, 16, 3, M_SP, 2048, 1792, 4, 7><<<224, 768, 0, stream>>>(
      w_sp, Zb + FH_, Zb, b_sp, Pb, Ub, nullptr, nullptr, nullptr);
  gemm_k<64, 64, 2, 2, 2, 2, 7, 4, M_OUT, 896, 512, 4, 8><<<256, 768, 0, stream>>>(
      Ub, w_out, Hb, nullptr, nullptr, out, nullptr, nullptr, nullptr);
}
```
